```python
import torch

def _softmax(x, axis=-1):
    return torch.softmax(x, dim=axis)
def _gelu(x):
    return torch.nn.functional.gelu(x, approximate="tanh")
def _silu(x):
    return torch.nn.functional.silu(x)
def _sigmoid(x):
    return torch.sigmoid(x)
def _layernorm(x, axis=-1, eps=1e-5):
    m = x.mean(dim=axis, keepdim=True)
    v = x.var(dim=axis, keepdim=True, unbiased=False)
    return (x - m) / torch.sqrt(v + eps)
def _scatter_add(tgt, idx, src, axis=0):
    return tgt.clone().index_add_(axis, idx, src)
def _segment_sum(src, seg, n):
    out = torch.zeros((n,) + src.shape[1:], dtype=src.dtype)
    return out.index_add_(0, seg, src)
def _pad(x, pw):
    flat = tuple(v for pair in reversed(pw) for v in pair)
    return torch.nn.functional.pad(x, flat)

kernel_name = 'sequential_preLN__linear_kernel_hedgehog__dense_activation_silu__prompt'

def setup_inputs():

    _w = (lambda a: (lambda t: t.to(torch.bfloat16) if t.is_floating_point() else t)(torch.as_tensor(a)))
    def _A_setup():
        B, S, D, H = 1, 2048, 448, 16
        Dh = D // H
        return {
            "x": _w((lambda *s: torch.randn(*s, dtype=torch.bfloat16) * 0.05)(B, S, D)),
            "wqkv": _w((lambda *s: torch.randn(*s, dtype=torch.bfloat16) * 0.2)(D, 3 * D)),
            "w_phi": _w((lambda *s: torch.randn(*s, dtype=torch.bfloat16) * 0.2)(H, Dh, Dh) * (Dh ** -0.5)),
            "wo": _w((lambda *s: torch.randn(*s, dtype=torch.bfloat16) * 0.05)(D, D)),
        }
    _A_in = _A_setup()

    def _B_setup():
        B, S, D, F = 1, 2048, 448, 1792
        return {
            "x": _w((lambda *s: torch.randn(*s, dtype=torch.bfloat16) * 0.05)(B, S, D)),
            "w1": _w((lambda *s: torch.randn(*s, dtype=torch.bfloat16) * 3.2)(D, F) * (D ** -0.5)),
            "w2": _w((lambda *s: torch.randn(*s, dtype=torch.bfloat16) * 0.1)(F, D) * (F ** -0.5)),
        }
    _B_in = _B_setup()

    return {
        "A_x": _A_in["x"],
        "A_wqkv": _A_in["wqkv"],
        "A_w_phi": _A_in["w_phi"],
        "A_wo": _A_in["wo"],
        "B_x": _B_in["x"],
        "B_w1": _B_in["w1"],
        "B_w2": _B_in["w2"],
    }

def reference(A_x, A_wqkv, A_w_phi, A_wo, B_x, B_w1, B_w2):
    def _A_ref(x, wqkv, w_phi, wo):
        B, S, D = x.shape
        H = 16; Dh = D // H
        qkv = (lambda x, *d: x.permute(*d))((x @ wqkv).reshape(B, S, 3, H, Dh), 2, 0, 3, 1, 4)
        q, k, v = qkv[0], qkv[1], qkv[2]
        pq = torch.einsum("bhsd,hdf->bhsf", q, w_phi)
        pk = torch.einsum("bhsd,hdf->bhsf", k, w_phi)
        phi_q = torch.concatenate([_softmax(pq, axis=-1), _softmax(-pq, axis=-1)], axis=-1)
        phi_k = torch.concatenate([_softmax(pk, axis=-1), _softmax(-pk, axis=-1)], axis=-1)
        kv = torch.einsum("bhsf,bhsd->bhfd", phi_k, v)
        z  = torch.einsum("bhsf->bhf", phi_k)
        num = torch.einsum("bhqf,bhfd->bhqd", phi_q, kv)
        den = torch.einsum("bhqf,bhf->bhq", phi_q, z)[..., None] + 1e-6
        o = num / den
        return (lambda x, *d: x.permute(*d))(o, 0, 2, 1, 3).reshape(B, S, D) @ wo

    def _B_ref(x, w1, w2):
        h = torch.einsum("bsd,df->bsf", x, w1)
        h = _silu(h)
        out = torch.einsum("bsf,fd->bsd", h, w2)
        return out

    x = A_x
    h = x + _A_ref(_layernorm(x, axis=-1), A_wqkv, A_w_phi, A_wo)
    h2 = h + _B_ref(_layernorm(h, axis=-1), B_w1, B_w2)
    return h2
```

```cpp
#include <hip/hip_runtime.h>
#include <hip/hip_bf16.h>
typedef unsigned short u16;
__device__ __forceinline__ float bf2f(u16 b){ return __uint_as_float(((unsigned)b)<<16); }
__device__ __forceinline__ u16 f2bf(float f){ unsigned u=__float_as_uint(f); u += 0x7fff + ((u>>16)&1); return (u16)(u>>16); }
constexpr int S=2048,D=448,H=16,DH=28,F=1792;
__global__ void k_ln_bf(const u16* x, float* y){
  int r=blockIdx.x; int l=threadIdx.x; float v[7]; float s=0;
  for(int i=0;i<7;i++){ v[i]=bf2f(x[r*D+l+64*i]); s+=v[i]; }
  for(int o=32;o;o>>=1) s+=__shfl_xor(s,o);
  float m=s/D; float q=0; for(int i=0;i<7;i++){ float d=v[i]-m; q+=d*d; }
  for(int o=32;o;o>>=1) q+=__shfl_xor(q,o);
  float rs=rsqrtf(q/D+1e-5f);
  for(int i=0;i<7;i++) y[r*D+l+64*i]=(v[i]-m)*rs;
}
__global__ void k_ln_f(const float* x, float* y){
  int r=blockIdx.x; int l=threadIdx.x; float v[7]; float s=0;
  for(int i=0;i<7;i++){ v[i]=x[r*D+l+64*i]; s+=v[i]; }
  for(int o=32;o;o>>=1) s+=__shfl_xor(s,o);
  float m=s/D; float q=0; for(int i=0;i<7;i++){ float d=v[i]-m; q+=d*d; }
  for(int o=32;o;o>>=1) q+=__shfl_xor(q,o);
  float rs=rsqrtf(q/D+1e-5f);
  for(int i=0;i<7;i++) y[r*D+l+64*i]=(v[i]-m)*rs;
}
template<int MODE> __global__ void k_gemm(const float* A,const u16* W,float* C,int N,int K,const u16* rb,const float* rf,u16* ob){
  __shared__ float As[16][65]; __shared__ float Ws[16][65];
  int tx=threadIdx.x&15, ty=threadIdx.x>>4; int m0=blockIdx.y*64, n0=blockIdx.x*64;
  float acc[4][4]={};
  for(int k0=0;k0<K;k0+=16){
    for(int i=threadIdx.x;i<1024;i+=256){ int m=i>>4,k=i&15; As[k][m]=A[(m0+m)*K+k0+k]; int kk=i>>6,n=i&63; Ws[kk][n]=bf2f(W[(k0+kk)*N+n0+n]); }
    __syncthreads();
    for(int k=0;k<16;k++){ float a[4],b[4]; for(int i=0;i<4;i++){a[i]=As[k][ty*4+i]; b[i]=Ws[k][tx*4+i];} for(int i=0;i<4;i++)for(int j=0;j<4;j++)acc[i][j]+=a[i]*b[j]; }
    __syncthreads();
  }
  for(int i=0;i<4;i++)for(int j=0;j<4;j++){ int m=m0+ty*4+i,n=n0+tx*4+j; float v=acc[i][j];
    if(MODE==0) C[m*N+n]=v; else if(MODE==1) C[m*N+n]=v/(1.f+__expf(-v)); else if(MODE==2) C[m*N+n]=v+bf2f(rb[m*N+n]); else ob[m*N+n]=f2bf(v+rf[m*N+n]); }
}
__global__ void k_phi(const float* qkv,const u16* wphi,float* phi){
  int idx=blockIdx.x*blockDim.x+threadIdx.x; if(idx>=2*S*H) return; int w=idx/(S*H); int s=(idx/H)%S; int h=idx%H;
  const float* q=qkv+s*3*D+w*D+h*DH; float p[DH]; float mx=-1e30f,mn=1e30f;
  #pragma unroll
  for(int f=0;f<DH;f++){ float a=0; for(int d=0;d<DH;d++) a+=q[d]*bf2f(wphi[(h*DH+d)*DH+f]); p[f]=a; mx=fmaxf(mx,a); mn=fminf(mn,a);}
  float s1=0,s2=0;
  #pragma unroll
  for(int f=0;f<DH;f++){ s1+=__expf(p[f]-mx); s2+=__expf(mn-p[f]); }
  float* o=phi+(size_t)idx*56;
  #pragma unroll
  for(int f=0;f<DH;f++){ o[f]=__expf(p[f]-mx)/s1; o[28+f]=__expf(mn-p[f])/s2; }
}
__global__ void k_kv(const float* qkv,const float* phi,float* kv){
  int h=blockIdx.x/56,f=blockIdx.x%56; int t=threadIdx.x; __shared__ float red[8][32];
  int d=t&31, g=t>>5; float a=0; const float* pk=phi+(size_t)S*H*56;
  for(int s=g;s<S;s+=8){ float p=pk[((size_t)s*H+h)*56+f]; float v= d<28? qkv[s*3*D+2*D+h*DH+d] : 1.f; a+=p*v; }
  red[g][d]=a; __syncthreads(); if(t<29){ float r=0; for(int i=0;i<8;i++) r+=red[i][t]; kv[(h*56+f)*29+t]=r; }
}
__global__ void k_o(const float* phi,const float* kv,float* o){
  int idx=blockIdx.x*blockDim.x+threadIdx.x; if(idx>=S*D) return; int s=idx/D,c=idx%D,h=c/DH,d=c%DH;
  const float* p=phi+((size_t)s*H+h)*56; float n=0,dn=0; for(int f=0;f<56;f++){ n+=p[f]*kv[(h*56+f)*29+d]; dn+=p[f]*kv[(h*56+f)*29+28]; }
  o[idx]=n/(dn+1e-6f);
}
extern "C" void kernel_launch(void* const* d_in, const int* in_sizes, int n_in,
                              void* d_out, int out_size, void* d_ws, size_t ws_size,
                              hipStream_t stream) {
  const u16* x=(const u16*)d_in[0]; const u16* wqkv=(const u16*)d_in[1]; const u16* wphi=(const u16*)d_in[2]; const u16* wo=(const u16*)d_in[3];
  const u16* w1=(const u16*)d_in[5]; const u16* w2=(const u16*)d_in[6];
  float* ws=(float*)d_ws; float* ln=ws; float* qkv=ln+S*D; float* phi=qkv+S*3*D; float* kv=phi+2*S*H*56; float* o=kv+32768; float* h=o+S*D; float* h1=h+S*D;
  k_ln_bf<<<S,64,0,stream>>>(x,ln);
  k_gemm<0><<<dim3(3*D/64,S/64),256,0,stream>>>(ln,wqkv,qkv,3*D,D,nullptr,nullptr,nullptr);
  k_phi<<<2*S*H/256,256,0,stream>>>(qkv,wphi,phi);
  k_kv<<<H*56,256,0,stream>>>(qkv,phi,kv);
  k_o<<<S*D/256,256,0,stream>>>(phi,kv,o);
  k_gemm<2><<<dim3(D/64,S/64),256,0,stream>>>(o,wo,h,D,D,x,nullptr,nullptr);
  k_ln_f<<<S,64,0,stream>>>(h,ln);
  k_gemm<1><<<dim3(F/64,S/64),256,0,stream>>>(ln,w1,h1,F,D,nullptr,nullptr,nullptr);
  k_gemm<3><<<dim3(D/64,S/64),256,0,stream>>>(h1,w2,nullptr,D,F,nullptr,h,(u16*)d_out);
}
```

```cpp
#include <hip/hip_runtime.h>
#include <hip/hip_bf16.h>

__global__ void __launch_bounds__(256, 1)
hh_block_kernel(const void* x, const void* wqkv, const void* wphi, const void* wo, const void* w1, const void* w2, void* out, void* ws) {
  asm volatile(R"ASM(
  s_load_dwordx2 s[4:5], %0, 0
  s_load_dwordx2 s[8:9], %0, 8
  s_load_dwordx2 s[12:13], %0, 16
  s_load_dwordx2 s[16:17], %0, 24
  s_load_dwordx2 s[20:21], %0, 32
  s_load_dwordx2 s[24:25], %0, 40
  s_load_dwordx2 s[28:29], %0, 48
  s_load_dwordx2 s[36:37], %0, 56
  s_getpc_b64 s[40:41]
Lhh_start:
  v_lshrrev_b32 v2, 6, %2
  s_lshr_b32 s50, %1, 3
  v_readfirstlane_b32 s49, v2
  s_mov_b32 s42, Lhh_end-Lhh_start
  s_mov_b32 s43, 0x20000
  v_lshlrev_b32 v1, 5, %2
  s_mov_b32 m0, 149504
  v_add_lshl_u32 v1, v1, s50, 7
  buffer_load_dword v1, s[40:43], 0 offen lds
  s_and_b32 s51, %1, 7
  s_lshr_b32 s52, s50, 4
  s_lshl_b32 s33, s51, 1
  s_add_u32 s33, s33, s52
  s_and_b32 s48, s50, 15
  s_lshl_b32 s52, s52, 2
  s_add_u32 s51, s51, s52
  s_add_u32 s51, s51, s48
  s_mul_i32 s52, s51, 37
  s_lshr_b32 s52, s52, 8
  s_mul_i32 s52, s52, 7
  s_sub_u32 s53, s51, s52
  s_lshl_b32 s51, s33, 2
  s_add_u32 s51, s51, s49
  s_mul_i32 s54, s51, 28672
  s_lshl_b32 s56, s49, 12
  s_mov_b32 s6, 1835008
  s_mov_b32 s7, 0x20000
  v_and_b32 v3, 63, %2
  v_lshrrev_b32 v1, 4, v3
  v_xor_b32 v1, v1, v3
  v_and_b32 v1, 7, v1
  v_lshlrev_b32 v1, 4, v1
  v_lshrrev_b32 v2, 3, v3
  v_mul_u32_u24 v2, 896, v2
  v_add_u32 v4, v1, v2
  v_xor_b32 v1, 64, v1
  v_add_u32 v1, v1, v2
  v_add_u32 v5, 6144, v1
  v_add_u32 v6, 12288, v4
  v_add_u32 v7, 12288, v5
  s_mul_i32 s55, s49, 43008
  s_mul_i32 s51, s48, 56
  s_add_u32 s55, s55, s51
  s_lshl_b32 s57, s49, 10
  s_mov_b32 s10, 1204224
  s_mov_b32 s11, 0x20000
  v_lshrrev_b32 v1, 2, v3
  v_and_b32 v2, 3, v3
  v_lshlrev_b32 v2, 4, v2
  v_mad_u32_u24 v9, v1, 56, v2
  v_min_u32 v2, 40, v2
  v_mul_u32_u24 v8, 2688, v1
  v_add_u32 v8, v8, v2
  s_lshl_b32 s51, s53, 7
  s_add_u32 s58, s54, s51
  s_mul_i32 s51, s53, 172032
  s_add_u32 s65, s55, s51
  s_add_u32 s53, s53, 1
  s_cmp_eq_u32 s53, 7
  s_cselect_b32 s53, 0, s53
  s_add_u32 m0, s56, 0
  s_waitcnt lgkmcnt(0)
  buffer_load_dwordx4 v4, s[4:7], s58 offen lds
  buffer_load_dwordx4 v5, s[4:7], s58 offen offset:1024 lds
  buffer_load_dwordx4 v6, s[4:7], s58 offen offset:2048 lds
  buffer_load_dwordx4 v7, s[4:7], s58 offen offset:3072 lds
  s_add_u32 m0, s57, 113792
  s_nop 0
  buffer_load_dwordx4 v8, s[8:11], s65 offen offset:896 lds
  s_lshl_b32 s51, s53, 7
  s_add_u32 s59, s54, s51
  s_mul_i32 s51, s53, 172032
  s_add_u32 s66, s55, s51
  s_add_u32 s53, s53, 1
  s_cmp_eq_u32 s53, 7
  s_cselect_b32 s53, 0, s53
  s_add_u32 s51, s48, 1
  s_mul_i32 s14, s51, 1568
  s_mov_b32 s15, 0x20000
  s_lshl_b32 s51, s53, 7
  s_add_u32 s60, s54, s51
  s_mul_i32 s51, s53, 172032
  s_add_u32 s67, s55, s51
  s_add_u32 s53, s53, 1
  s_cmp_eq_u32 s53, 7
  s_cselect_b32 s53, 0, s53
  s_mov_b32 s38, 67108864
  s_mov_b32 s39, 0x20000
  s_mov_b32 s18, 401408
  s_add_u32 m0, s56, 16384
  s_mov_b32 s19, 0x20000
  buffer_load_dwordx4 v4, s[4:7], s59 offen lds
  s_mov_b32 s22, 1605632
  s_mov_b32 s23, 0x20000
  s_mov_b32 s26, 1605632
  s_mov_b32 s27, 0x20000
  s_mov_b32 s30, 1835008
  s_mov_b32 s31, 0x20000
  s_lshl_b32 s51, s53, 7
  s_add_u32 s61, s54, s51
  s_mul_i32 s51, s53, 172032
  s_add_u32 s68, s55, s51
  s_add_u32 s53, s53, 1
  s_cmp_eq_u32 s53, 7
  s_cselect_b32 s53, 0, s53
  s_lshl_b32 s51, s53, 7
  s_add_u32 s62, s54, s51
  s_mul_i32 s51, s53, 172032
  s_add_u32 s69, s55, s51
  s_add_u32 s53, s53, 1
  s_cmp_eq_u32 s53, 7
  s_cselect_b32 s53, 0, s53
  s_lshl_b32 s51, s53, 7
  s_add_u32 s63, s54, s51
  s_mul_i32 s51, s53, 172032
  s_add_u32 s70, s55, s51
  s_add_u32 s53, s53, 1
  s_cmp_eq_u32 s53, 7
  s_cselect_b32 s53, 0, s53
  s_lshl_b32 s51, s53, 7
  s_add_u32 s64, s54, s51
  s_mul_i32 s51, s53, 172032
  s_add_u32 s71, s55, s51
  s_add_u32 s53, s53, 1
  s_cmp_eq_u32 s53, 7
  s_cselect_b32 s53, 0, s53
  v_lshlrev_b32 v12, 4, v3
  s_mov_b32 s46, 0
  s_mov_b32 s47, -1
  v_and_b32 v13, 31, v3
  v_lshrrev_b32 v14, 5, v3
  v_bfe_u32 v15, v3, 2, 2
  v_bfe_u32 v1, v3, 4, 1
  v_and_b32 v2, 3, v3
  v_lshlrev_b32 v2, 3, v2
  v_lshl_add_u32 v16, v1, 5, v2
  v_bfe_u32 v1, v3, 1, 3
  v_xor_b32 v1, v1, v14
  v_lshlrev_b32 v1, 4, v1
  v_lshl_add_u32 v1, v13, 7, v1
  v_add_u32 v1, s56, v1
  v_lshrrev_b32 v2, 3, v3
  buffer_load_dwordx4 v5, s[4:7], s59 offen offset:1024 lds
  v_and_b32 v2, v2, v3
  v_and_b32 v2, 2, v2
  v_lshlrev_b32 v2, 2, v2
  v_xor_b32 v21, v2, v16
  v_lshl_add_u32 v2, v14, 3, v15
  v_lshl_add_u32 v2, v2, 6, v21
  v_add_u32 v2, 114688, v2
  s_add_u32 s51, s49, 0
  s_and_b32 s51, s51, 3
  s_lshl_b32 s52, s51, 5
  s_lshl_b32 s51, s51, 10
  v_xor_b32 v17, s52, v1
  v_add_u32 v21, s51, v2
  s_add_u32 s51, s49, 1
  s_and_b32 s51, s51, 3
  s_lshl_b32 s52, s51, 5
  s_lshl_b32 s51, s51, 10
  v_xor_b32 v18, s52, v1
  v_add_u32 v22, s51, v2
  s_add_u32 s51, s49, 2
  s_and_b32 s51, s51, 3
  s_lshl_b32 s52, s51, 5
  s_lshl_b32 s51, s51, 10
  v_xor_b32 v19, s52, v1
  v_add_u32 v23, s51, v2
  s_add_u32 s51, s49, 3
  s_and_b32 s51, s51, 3
  s_lshl_b32 s52, s51, 5
  s_lshl_b32 s51, s51, 10
  v_xor_b32 v20, s52, v1
  v_add_u32 v24, s51, v2
  v_lshl_add_u32 v25, v14, 2, v15
  s_mov_b32 s72, 0x3f803f80
  v_mov_b32 v162, 0
  v_mov_b32 v163, 0
  v_mov_b32 v164, 0
  v_mov_b32 v165, 0
  v_mov_b32 v166, 0
  v_mov_b32 v167, 0
  v_mov_b32 v170, 0
  v_mov_b32 v171, 0
  v_mov_b32 v174, 0
  v_mov_b32 v175, 0
  v_accvgpr_write_b32 a240, v4
  v_accvgpr_write_b32 a241, v5
  v_mul_u32_u24 v1, 136, v13
  v_lshl_add_u32 v1, v14, 3, v1
  s_mul_i32 s51, s49, 4352
  s_add_u32 s51, s51, 65536
  v_add_u32 v1, s51, v1
  buffer_load_dwordx4 v6, s[4:7], s59 offen offset:2048 lds
  v_accvgpr_write_b32 a242, v1
  v_lshrrev_b32 v146, 4, v3
  v_and_b32 v147, 15, v3
  v_lshl_add_u32 v1, v146, 3, v15
  v_mul_u32_u24 v1, 136, v1
  v_and_b32 v2, 3, v3
  v_lshl_add_u32 v1, v2, 3, v1
  s_lshl_b32 s51, s49, 5
  s_add_u32 s51, s51, 65536
  v_add_u32 v1, s51, v1
  v_accvgpr_write_b32 a243, v1
  s_lshl_b32 s51, s49, 2
  v_xor_b32 v2, s51, v147
  v_lshlrev_b32 v2, 4, v2
  v_lshl_add_u32 v2, v13, 8, v2
  v_lshl_add_u32 v2, v14, 3, v2
  v_add_u32 v2, 86016, v2
  v_accvgpr_write_b32 a244, v2
  v_xor_b32 v2, v146, v147
  v_lshlrev_b32 v2, 4, v2
  v_lshl_add_u32 v2, v147, 8, v2
  v_add_u32 v2, 86016, v2
  v_accvgpr_write_b32 a245, v2
  v_and_b32 v1, 1, v146
  v_lshlrev_b32 v2, 9, v1
  v_lshl_add_u32 v2, v14, 8, v2
  v_lshl_add_u32 v2, v147, 4, v2
  v_accvgpr_write_b32 a246, v2
  v_lshlrev_b32 v2, 8, v14
  v_lshl_add_u32 v2, v147, 4, v2
  v_lshl_add_u32 v2, v1, 3, v2
  s_add_u32 s52, s57, 147456
  v_add_u32 v2, s52, v2
  v_accvgpr_write_b32 a247, v2
  v_lshlrev_b32 v1, 3, v1
  v_sub_u32 v1, v147, v1
  v_and_b32 v2, 1, v1
  v_lshlrev_b32 v2, 4, v2
  v_mov_b32 v148, 0x3f80
  v_lshlrev_b32 v2, v2, v148
  v_ashrrev_i32 v1, 1, v1
  v_cmp_eq_u32 vcc, 0, v1
  s_nop 1
  v_cndmask_b32 v148, 0, v2, vcc
  v_accvgpr_write_b32 a252, v148
  v_cmp_eq_u32 vcc, 1, v1
  s_nop 1
  v_cndmask_b32 v148, 0, v2, vcc
  v_accvgpr_write_b32 a253, v148
  v_cmp_eq_u32 vcc, 2, v1
  s_nop 1
  v_cndmask_b32 v148, 0, v2, vcc
  v_accvgpr_write_b32 a254, v148
  buffer_load_dwordx4 v7, s[4:7], s59 offen offset:3072 lds
  v_cmp_eq_u32 vcc, 3, v1
  s_nop 1
  v_cndmask_b32 v148, 0, v2, vcc
  v_accvgpr_write_b32 a255, v148
  v_lshrrev_b32 v1, 3, v13
  v_add_u32 v1, s51, v1
  v_xor_b32 v1, 12, v1
  v_lshlrev_b32 v1, 4, v1
  v_and_b32 v2, 7, v13
  v_lshl_add_u32 v1, v2, 1, v1
  v_add_u32 v1, 93184, v1
  v_accvgpr_write_b32 a248, v1
  v_lshlrev_b32 v1, 2, v146
  v_xor_b32 v1, v1, v147
  v_lshlrev_b32 v1, 4, v1
  v_mul_u32_u24 v2, 3584, v146
  v_add_u32 v1, v1, v2
  v_accvgpr_write_b32 a249, v1
  v_lshrrev_b32 v1, 2, v3
  v_and_b32 v1, 4, v1
  v_xor_b32 v1, v1, v3
  v_and_b32 v1, 7, v1
  v_lshlrev_b32 v1, 4, v1
  v_lshrrev_b32 v2, 3, v3
  v_mul_u32_u24 v2, 896, v2
  v_add_u32 v1, v1, v2
  v_accvgpr_write_b32 a250, v1
  s_cmp_gt_u32 s48, 2
  s_cselect_b32 s51, 1, 0
  s_sub_u32 s73, s48, s51
  s_cmp_gt_u32 s48, 13
  s_cselect_b32 s51, 1, 0
  s_sub_u32 s73, s73, s51
  s_cmp_ge_u32 s73, 7
  s_cselect_b32 s74, 1, 0
  s_mul_i32 s51, s74, 7
  s_sub_u32 s75, s73, s51
  s_lshr_b32 s76, 57339, s48
  s_and_b32 s76, s76, 1
  s_and_b32 s77, s49, 1
  s_lshr_b32 s78, s49, 1
  s_lshl_b32 s79, s74, 1
  s_add_u32 s79, s79, s77
  s_lshl_b32 s51, s77, 12
  s_add_u32 s51, s51, 155648
  s_lshl_b32 s52, s78, 6
  v_bfe_u32 v1, v3, 1, 3
  v_lshlrev_b32 v1, 4, v1
  v_xor_b32 v1, s52, v1
  v_lshl_add_u32 v1, v13, 7, v1
  s_add_u32 m0, s57, 117888
  v_lshl_add_u32 v1, v14, 3, v1
  buffer_load_dwordx4 v8, s[8:11], s66 offen offset:896 lds
  v_add_u32 v1, s51, v1
  v_accvgpr_write_b32 a251, v1
  s_lshl_b32 s51, s48, 4
  s_add_u32 s51, s51, s33
  s_lshl_b32 s51, s51, 12
  s_add_u32 s51, s51, s57
  s_add_u32 s80, s51, 1048576
  s_lshl_b32 s51, s48, 8
  s_lshl_b32 s52, s33, 3
  s_add_u32 s51, s51, s52
  s_add_u32 s81, s51, 4096
  s_lshl_b32 s51, s33, 12
  s_lshl_b32 s52, s48, 3
  s_add_u32 s51, s51, s52
  s_add_u32 s82, s51, 69632
  s_lshl_b32 s51, s33, 4
  s_add_u32 s51, s51, s48
  s_lshl_b32 s51, s51, 8
  s_add_u32 s83, s51, 4096
  s_lshl_b32 s51, s33, 2
  s_add_u32 s51, s51, s49
  s_lshl_b32 s51, s51, 4
  s_add_u32 s51, s51, s48
  s_lshl_b32 s51, s51, 11
  s_add_u32 s84, s51, 2097152
  s_lshl_b32 s85, s48, 16
  s_and_b32 s35, s49, 1
  s_mul_i32 s54, s48, 1568
  s_lshl_b32 s34, s35, 10
  s_add_u32 m0, s34, 147456
  s_mul_i32 s35, s35, 896
  s_add_u32 s35, s35, s54
  buffer_load_dwordx4 v9, s[12:15], s35 offen lds
  s_add_u32 s85, s85, s57
  s_add_u32 s85, s85, 1048576
  s_and_b32 s51, s33, 1
  s_lshl_b32 s51, s51, 15
  s_add_u32 s85, s85, s51
  s_lshl_b32 s51, s33, 1
  s_add_u32 s51, s51, s74
  s_lshl_b32 s51, s51, 2
  s_add_u32 s51, s51, s49
  s_mul_i32 s51, s51, 14336
  v_mov_b32 v10, 0
  buffer_load_dwordx2 v[10:11], v10, s[36:39], 0 offen sc1
  s_lshl_b32 s86, s75, 7
  s_add_u32 s86, s86, s51
  s_waitcnt vmcnt(7)
  s_barrier
  s_add_u32 m0, s56, 32768
  s_nop 0
  buffer_load_dwordx4 v4, s[4:7], s60 offen lds
  ds_read_b128 v[26:29], v17 offset:0
  ds_read_b128 v[30:33], v18 offset:0
  ds_read_b128 v[34:37], v19 offset:0
  ds_read_b128 v[38:41], v20 offset:0
  buffer_load_dwordx4 v5, s[4:7], s60 offen offset:1024 lds
  ds_read_b64_tr_b16 v[138:139], v21 offset:0
  buffer_load_dwordx4 v6, s[4:7], s60 offen offset:2048 lds
  ds_read_b64_tr_b16 v[140:141], v21 offset:256
  ds_read_b64_tr_b16 a[0:1], v22 offset:0
  ds_read_b64_tr_b16 a[2:3], v22 offset:256
  buffer_load_dwordx4 v7, s[4:7], s60 offen offset:3072 lds
  ds_read_b64_tr_b16 a[4:5], v23 offset:0
  ds_read_b64_tr_b16 a[6:7], v23 offset:256
  ds_read_b64_tr_b16 a[8:9], v24 offset:0
  ds_read_b64_tr_b16 a[10:11], v24 offset:256
  s_add_u32 m0, s57, 121984
  s_nop 0
  buffer_load_dwordx4 v8, s[8:11], s67 offen offset:896 lds
  s_waitcnt lgkmcnt(10)
  v_dot2c_f32_bf16 v162, s72, v26
  v_dot2c_f32_bf16 v163, s72, v27
  v_dot2c_f32_bf16 v162, s72, v28
  s_add_u32 m0, s56, 49152
  s_nop 0
  buffer_load_dwordx4 v4, s[4:7], s61 offen lds
  v_dot2c_f32_bf16 v163, s72, v29
  v_dot2c_f32_bf16 v164, v26, v26
  v_dot2c_f32_bf16 v165, v27, v27
  buffer_load_dwordx4 v5, s[4:7], s61 offen offset:1024 lds
  v_dot2c_f32_bf16 v164, v28, v28
  v_dot2c_f32_bf16 v165, v29, v29
  v_dot2c_f32_bf16 v162, s72, v30
  buffer_load_dwordx4 v6, s[4:7], s61 offen offset:2048 lds
  v_dot2c_f32_bf16 v163, s72, v31
  v_dot2c_f32_bf16 v162, s72, v32
  v_dot2c_f32_bf16 v163, s72, v33
  buffer_load_dwordx4 v7, s[4:7], s61 offen offset:3072 lds
  v_dot2c_f32_bf16 v164, v30, v30
  v_dot2c_f32_bf16 v165, v31, v31
  v_dot2c_f32_bf16 v164, v32, v32
  s_add_u32 m0, s57, 126080
  s_nop 0
  buffer_load_dwordx4 v8, s[8:11], s68 offen offset:896 lds
  v_dot2c_f32_bf16 v165, v33, v33
  s_waitcnt lgkmcnt(8)
  v_dot2c_f32_bf16 v162, s72, v34
  v_dot2c_f32_bf16 v163, s72, v35
  v_dot2c_f32_bf16 v162, s72, v36
  v_dot2c_f32_bf16 v163, s72, v37
  v_dot2c_f32_bf16 v164, v34, v34
  v_dot2c_f32_bf16 v165, v35, v35
  v_dot2c_f32_bf16 v164, v36, v36
  v_dot2c_f32_bf16 v165, v37, v37
  v_dot2c_f32_bf16 v162, s72, v38
  v_dot2c_f32_bf16 v163, s72, v39
  v_dot2c_f32_bf16 v162, s72, v40
  v_dot2c_f32_bf16 v163, s72, v41
  v_dot2c_f32_bf16 v164, v38, v38
  v_dot2c_f32_bf16 v165, v39, v39
  v_dot2c_f32_bf16 v164, v40, v40
  v_dot2c_f32_bf16 v165, v41, v41
  s_waitcnt lgkmcnt(6)
  v_dot2c_f32_bf16 v166, s72, v138
  v_dot2c_f32_bf16 v167, s72, v139
  v_dot2c_f32_bf16 v166, s72, v140
  v_dot2c_f32_bf16 v167, s72, v141
  s_waitcnt vmcnt(12)
  s_barrier
  v_mfma_f32_32x32x16_bf16 v[146:161], v[138:141], v[26:29], 0
  ds_read_b128 v[42:45], v17 offset:16384
  ds_read_b128 v[46:49], v18 offset:16384
  s_waitcnt lgkmcnt(2)
  v_mfma_f32_32x32x16_bf16 v[146:161], a[0:3], v[30:33], v[146:161]
  ds_read_b128 v[50:53], v19 offset:16384
  ds_read_b128 v[54:57], v20 offset:16384
  s_add_u32 m0, s56, 65536
  s_nop 0
  buffer_load_dwordx4 v4, s[4:7], s62 offen lds
  v_mfma_f32_32x32x16_bf16 v[146:161], a[4:7], v[34:37], v[146:161]
  ds_read_b64_tr_b16 v[142:143], v21 offset:4096
  ds_read_b64_tr_b16 v[144:145], v21 offset:4352
  v_mfma_f32_32x32x16_bf16 v[146:161], a[8:11], v[38:41], v[146:161]
  ds_read_b64_tr_b16 a[12:13], v22 offset:4096
  ds_read_b64_tr_b16 a[14:15], v22 offset:4352
  ds_read_b64_tr_b16 a[16:17], v23 offset:4096
  ds_read_b64_tr_b16 a[18:19], v23 offset:4352
  ds_read_b64_tr_b16 a[20:21], v24 offset:4096
  ds_read_b64_tr_b16 a[22:23], v24 offset:4352
  buffer_load_dwordx4 v5, s[4:7], s62 offen offset:1024 lds
  s_waitcnt lgkmcnt(10)
  v_dot2c_f32_bf16 v162, s72, v42
  v_dot2c_f32_bf16 v163, s72, v43
  v_dot2c_f32_bf16 v162, s72, v44
  v_dot2c_f32_bf16 v163, s72, v45
  v_dot2c_f32_bf16 v164, v42, v42
  v_dot2c_f32_bf16 v165, v43, v43
  v_dot2c_f32_bf16 v164, v44, v44
  v_dot2c_f32_bf16 v165, v45, v45
  v_dot2c_f32_bf16 v162, s72, v46
  v_dot2c_f32_bf16 v163, s72, v47
  v_dot2c_f32_bf16 v162, s72, v48
  v_dot2c_f32_bf16 v163, s72, v49
  buffer_load_dwordx4 v6, s[4:7], s62 offen offset:2048 lds
  v_dot2c_f32_bf16 v164, v46, v46
  v_dot2c_f32_bf16 v165, v47, v47
  v_dot2c_f32_bf16 v164, v48, v48
  v_dot2c_f32_bf16 v165, v49, v49
  s_waitcnt lgkmcnt(8)
  v_dot2c_f32_bf16 v162, s72, v50
  v_dot2c_f32_bf16 v163, s72, v51
  v_dot2c_f32_bf16 v162, s72, v52
  v_dot2c_f32_bf16 v163, s72, v53
  v_dot2c_f32_bf16 v164, v50, v50
  v_dot2c_f32_bf16 v165, v51, v51
  v_dot2c_f32_bf16 v164, v52, v52
  v_dot2c_f32_bf16 v165, v53, v53
  buffer_load_dwordx4 v7, s[4:7], s62 offen offset:3072 lds
  v_dot2c_f32_bf16 v162, s72, v54
  v_dot2c_f32_bf16 v163, s72, v55
  v_dot2c_f32_bf16 v162, s72, v56
  v_dot2c_f32_bf16 v163, s72, v57
  v_dot2c_f32_bf16 v164, v54, v54
  v_dot2c_f32_bf16 v165, v55, v55
  v_dot2c_f32_bf16 v164, v56, v56
  v_dot2c_f32_bf16 v165, v57, v57
  s_waitcnt lgkmcnt(6)
  v_dot2c_f32_bf16 v166, s72, v142
  v_dot2c_f32_bf16 v167, s72, v143
  v_dot2c_f32_bf16 v166, s72, v144
  v_dot2c_f32_bf16 v167, s72, v145
  s_add_u32 m0, s57, 130176
  s_nop 0
  buffer_load_dwordx4 v8, s[8:11], s69 offen offset:896 lds
  s_waitcnt vmcnt(10)
  s_barrier
  v_mfma_f32_32x32x16_bf16 v[146:161], v[142:145], v[42:45], v[146:161]
  ds_read_b128 v[58:61], v17 offset:32768
  ds_read_b128 v[62:65], v18 offset:32768
  s_waitcnt lgkmcnt(2)
  v_mfma_f32_32x32x16_bf16 v[146:161], a[12:15], v[46:49], v[146:161]
  ds_read_b128 v[66:69], v19 offset:32768
  ds_read_b128 v[70:73], v20 offset:32768
  s_add_u32 m0, s56, 81920
  s_nop 0
  buffer_load_dwordx4 v4, s[4:7], s63 offen lds
  v_mfma_f32_32x32x16_bf16 v[146:161], a[16:19], v[50:53], v[146:161]
  ds_read_b64_tr_b16 v[138:139], v21 offset:8192
  ds_read_b64_tr_b16 v[140:141], v21 offset:8448
  v_mfma_f32_32x32x16_bf16 v[146:161], a[20:23], v[54:57], v[146:161]
  ds_read_b64_tr_b16 a[0:1], v22 offset:8192
  ds_read_b64_tr_b16 a[2:3], v22 offset:8448
  ds_read_b64_tr_b16 a[4:5], v23 offset:8192
  ds_read_b64_tr_b16 a[6:7], v23 offset:8448
  ds_read_b64_tr_b16 a[8:9], v24 offset:8192
  ds_read_b64_tr_b16 a[10:11], v24 offset:8448
  buffer_load_dwordx4 v5, s[4:7], s63 offen offset:1024 lds
  s_waitcnt lgkmcnt(10)
  v_dot2c_f32_bf16 v162, s72, v58
  v_dot2c_f32_bf16 v163, s72, v59
  v_dot2c_f32_bf16 v162, s72, v60
  v_dot2c_f32_bf16 v163, s72, v61
  v_dot2c_f32_bf16 v164, v58, v58
  v_dot2c_f32_bf16 v165, v59, v59
  v_dot2c_f32_bf16 v164, v60, v60
  v_dot2c_f32_bf16 v165, v61, v61
  v_dot2c_f32_bf16 v162, s72, v62
  v_dot2c_f32_bf16 v163, s72, v63
  v_dot2c_f32_bf16 v162, s72, v64
  v_dot2c_f32_bf16 v163, s72, v65
  buffer_load_dwordx4 v6, s[4:7], s63 offen offset:2048 lds
  v_dot2c_f32_bf16 v164, v62, v62
  v_dot2c_f32_bf16 v165, v63, v63
  v_dot2c_f32_bf16 v164, v64, v64
  v_dot2c_f32_bf16 v165, v65, v65
  s_waitcnt lgkmcnt(8)
  v_dot2c_f32_bf16 v162, s72, v66
  v_dot2c_f32_bf16 v163, s72, v67
  v_dot2c_f32_bf16 v162, s72, v68
  v_dot2c_f32_bf16 v163, s72, v69
  v_dot2c_f32_bf16 v164, v66, v66
  v_dot2c_f32_bf16 v165, v67, v67
  v_dot2c_f32_bf16 v164, v68, v68
  v_dot2c_f32_bf16 v165, v69, v69
  buffer_load_dwordx4 v7, s[4:7], s63 offen offset:3072 lds
  v_dot2c_f32_bf16 v162, s72, v70
  v_dot2c_f32_bf16 v163, s72, v71
  v_dot2c_f32_bf16 v162, s72, v72
  v_dot2c_f32_bf16 v163, s72, v73
  v_dot2c_f32_bf16 v164, v70, v70
  v_dot2c_f32_bf16 v165, v71, v71
  v_dot2c_f32_bf16 v164, v72, v72
  v_dot2c_f32_bf16 v165, v73, v73
  s_waitcnt lgkmcnt(6)
  v_dot2c_f32_bf16 v166, s72, v138
  v_dot2c_f32_bf16 v167, s72, v139
  v_dot2c_f32_bf16 v166, s72, v140
  v_dot2c_f32_bf16 v167, s72, v141
  s_add_u32 m0, s57, 134272
  s_nop 0
  buffer_load_dwordx4 v8, s[8:11], s70 offen offset:896 lds
  s_waitcnt vmcnt(10)
  s_barrier
  v_mfma_f32_32x32x16_bf16 v[146:161], v[138:141], v[58:61], v[146:161]
  ds_read_b128 v[74:77], v17 offset:49152
  ds_read_b128 v[78:81], v18 offset:49152
  s_waitcnt lgkmcnt(2)
  v_mfma_f32_32x32x16_bf16 v[146:161], a[0:3], v[62:65], v[146:161]
  ds_read_b128 v[82:85], v19 offset:49152
  ds_read_b128 v[86:89], v20 offset:49152
  s_add_u32 m0, s56, 98304
  s_nop 0
  buffer_load_dwordx4 v4, s[4:7], s64 offen lds
  v_mfma_f32_32x32x16_bf16 v[146:161], a[4:7], v[66:69], v[146:161]
  ds_read_b64_tr_b16 v[142:143], v21 offset:12288
  ds_read_b64_tr_b16 v[144:145], v21 offset:12544
  v_mfma_f32_32x32x16_bf16 v[146:161], a[8:11], v[70:73], v[146:161]
  ds_read_b64_tr_b16 a[12:13], v22 offset:12288
  ds_read_b64_tr_b16 a[14:15], v22 offset:12544
  ds_read_b64_tr_b16 a[16:17], v23 offset:12288
  ds_read_b64_tr_b16 a[18:19], v23 offset:12544
  ds_read_b64_tr_b16 a[20:21], v24 offset:12288
  ds_read_b64_tr_b16 a[22:23], v24 offset:12544
  buffer_load_dwordx4 v5, s[4:7], s64 offen offset:1024 lds
  s_waitcnt lgkmcnt(10)
  v_dot2c_f32_bf16 v162, s72, v74
  v_dot2c_f32_bf16 v163, s72, v75
  v_dot2c_f32_bf16 v162, s72, v76
  v_dot2c_f32_bf16 v163, s72, v77
  v_dot2c_f32_bf16 v164, v74, v74
  v_dot2c_f32_bf16 v165, v75, v75
  v_dot2c_f32_bf16 v164, v76, v76
  v_dot2c_f32_bf16 v165, v77, v77
  v_dot2c_f32_bf16 v162, s72, v78
  v_dot2c_f32_bf16 v163, s72, v79
  v_dot2c_f32_bf16 v162, s72, v80
  v_dot2c_f32_bf16 v163, s72, v81
  v_dot2c_f32_bf16 v164, v78, v78
  v_dot2c_f32_bf16 v165, v79, v79
  v_dot2c_f32_bf16 v164, v80, v80
  v_dot2c_f32_bf16 v165, v81, v81
  buffer_load_dwordx4 v6, s[4:7], s64 offen offset:2048 lds
  s_waitcnt lgkmcnt(8)
  v_dot2c_f32_bf16 v162, s72, v82
  v_dot2c_f32_bf16 v163, s72, v83
  v_dot2c_f32_bf16 v162, s72, v84
  v_dot2c_f32_bf16 v163, s72, v85
  v_dot2c_f32_bf16 v164, v82, v82
  v_dot2c_f32_bf16 v165, v83, v83
  v_dot2c_f32_bf16 v164, v84, v84
  v_dot2c_f32_bf16 v165, v85, v85
  v_dot2c_f32_bf16 v162, s72, v86
  v_dot2c_f32_bf16 v163, s72, v87
  v_dot2c_f32_bf16 v162, s72, v88
  v_dot2c_f32_bf16 v163, s72, v89
  v_dot2c_f32_bf16 v164, v86, v86
  v_dot2c_f32_bf16 v165, v87, v87
  v_dot2c_f32_bf16 v164, v88, v88
  v_dot2c_f32_bf16 v165, v89, v89
  buffer_load_dwordx4 v7, s[4:7], s64 offen offset:3072 lds
  s_waitcnt lgkmcnt(6)
  v_dot2c_f32_bf16 v166, s72, v142
  v_dot2c_f32_bf16 v167, s72, v143
  v_dot2c_f32_bf16 v166, s72, v144
  v_dot2c_f32_bf16 v167, s72, v145
  s_add_u32 m0, s57, 138368
  s_nop 0
  buffer_load_dwordx4 v8, s[8:11], s71 offen offset:896 lds
  v_add_u32 v17, 65536, v17
  v_add_u32 v18, 65536, v18
  v_add_u32 v19, 65536, v19
  v_add_u32 v20, 65536, v20
  v_readfirstlane_b32 s44, v10
  v_readfirstlane_b32 s45, v11
  s_add_u32 s44, s44, 1
  s_addc_u32 s45, s45, 0
  v_lshl_add_u32 v1, v25, 6, v16
  v_add_u32 v1, 147456, v1
  ds_read_b64_tr_b16 v[178:179], v1 offset:0
  ds_read_b64_tr_b16 v[180:181], v1 offset:512
  ds_read_b64_tr_b16 v[182:183], v1 offset:1024
  ds_read_b64_tr_b16 v[184:185], v1 offset:1536
  s_waitcnt vmcnt(10)
  s_barrier
  v_mfma_f32_32x32x16_bf16 v[146:161], v[142:145], v[74:77], v[146:161]
  ds_read_b128 v[90:93], v17 offset:0
  ds_read_b128 v[94:97], v18 offset:0
  s_waitcnt lgkmcnt(6)
  v_mfma_f32_32x32x16_bf16 v[146:161], a[12:15], v[78:81], v[146:161]
  ds_read_b128 v[98:101], v19 offset:0
  ds_read_b128 v[102:105], v20 offset:0
  s_add_u32 m0, s57, 30976
  s_nop 0
  buffer_load_dwordx4 v8, s[8:11], s65 offen offset:1792 lds
  v_mfma_f32_32x32x16_bf16 v[146:161], a[16:19], v[82:85], v[146:161]
  ds_read_b64_tr_b16 v[138:139], v21 offset:16384
  ds_read_b64_tr_b16 v[140:141], v21 offset:16640
  v_mfma_f32_32x32x16_bf16 v[146:161], a[20:23], v[86:89], v[146:161]
  ds_read_b64_tr_b16 a[0:1], v22 offset:16384
  ds_read_b64_tr_b16 a[2:3], v22 offset:16640
  ds_read_b64_tr_b16 a[4:5], v23 offset:16384
  ds_read_b64_tr_b16 a[6:7], v23 offset:16640
  ds_read_b64_tr_b16 a[8:9], v24 offset:16384
  ds_read_b64_tr_b16 a[10:11], v24 offset:16640
  s_waitcnt lgkmcnt(10)
  v_dot2c_f32_bf16 v162, s72, v90
  v_dot2c_f32_bf16 v163, s72, v91
  v_dot2c_f32_bf16 v162, s72, v92
  v_dot2c_f32_bf16 v163, s72, v93
  v_dot2c_f32_bf16 v164, v90, v90
  v_dot2c_f32_bf16 v165, v91, v91
  v_dot2c_f32_bf16 v164, v92, v92
  v_dot2c_f32_bf16 v165, v93, v93
  v_dot2c_f32_bf16 v162, s72, v94
  v_dot2c_f32_bf16 v163, s72, v95
  v_dot2c_f32_bf16 v162, s72, v96
  v_dot2c_f32_bf16 v163, s72, v97
  v_dot2c_f32_bf16 v164, v94, v94
  v_dot2c_f32_bf16 v165, v95, v95
  v_dot2c_f32_bf16 v164, v96, v96
  v_dot2c_f32_bf16 v165, v97, v97
  s_waitcnt lgkmcnt(8)
  v_dot2c_f32_bf16 v162, s72, v98
  v_dot2c_f32_bf16 v163, s72, v99
  s_add_u32 m0, s57, 35072
  s_nop 0
  buffer_load_dwordx4 v8, s[8:11], s66 offen offset:1792 lds
  v_dot2c_f32_bf16 v162, s72, v100
  v_dot2c_f32_bf16 v163, s72, v101
  v_dot2c_f32_bf16 v164, v98, v98
  v_dot2c_f32_bf16 v165, v99, v99
  v_dot2c_f32_bf16 v164, v100, v100
  v_dot2c_f32_bf16 v165, v101, v101
  v_dot2c_f32_bf16 v162, s72, v102
  v_dot2c_f32_bf16 v163, s72, v103
  v_dot2c_f32_bf16 v162, s72, v104
  v_dot2c_f32_bf16 v163, s72, v105
  v_dot2c_f32_bf16 v164, v102, v102
  v_dot2c_f32_bf16 v165, v103, v103
  v_dot2c_f32_bf16 v164, v104, v104
  v_dot2c_f32_bf16 v165, v105, v105
  s_waitcnt lgkmcnt(6)
  v_dot2c_f32_bf16 v166, s72, v138
  v_dot2c_f32_bf16 v167, s72, v139
  v_dot2c_f32_bf16 v166, s72, v140
  v_dot2c_f32_bf16 v167, s72, v141
  s_waitcnt vmcnt(7)
  s_barrier
  v_mfma_f32_32x32x16_bf16 v[146:161], v[138:141], v[90:93], v[146:161]
  ds_read_b128 v[106:109], v17 offset:16384
  ds_read_b128 v[110:113], v18 offset:16384
  s_waitcnt lgkmcnt(2)
  v_mfma_f32_32x32x16_bf16 v[146:161], a[0:3], v[94:97], v[146:161]
  ds_read_b128 v[114:117], v19 offset:16384
  ds_read_b128 v[118:121], v20 offset:16384
  s_add_u32 m0, s57, 39168
  s_nop 0
  buffer_load_dwordx4 v8, s[8:11], s67 offen offset:1792 lds
  v_mfma_f32_32x32x16_bf16 v[146:161], a[4:7], v[98:101], v[146:161]
  ds_read_b64_tr_b16 v[142:143], v21 offset:20480
  ds_read_b64_tr_b16 v[144:145], v21 offset:20736
  v_mfma_f32_32x32x16_bf16 v[146:161], a[8:11], v[102:105], v[146:161]
  ds_read_b64_tr_b16 a[12:13], v22 offset:20480
  ds_read_b64_tr_b16 a[14:15], v22 offset:20736
  ds_read_b64_tr_b16 a[16:17], v23 offset:20480
  ds_read_b64_tr_b16 a[18:19], v23 offset:20736
  ds_read_b64_tr_b16 a[20:21], v24 offset:20480
  ds_read_b64_tr_b16 a[22:23], v24 offset:20736
  s_waitcnt lgkmcnt(10)
  v_dot2c_f32_bf16 v162, s72, v106
  v_dot2c_f32_bf16 v163, s72, v107
  v_dot2c_f32_bf16 v162, s72, v108
  v_dot2c_f32_bf16 v163, s72, v109
  v_dot2c_f32_bf16 v164, v106, v106
  v_dot2c_f32_bf16 v165, v107, v107
  v_dot2c_f32_bf16 v164, v108, v108
  v_dot2c_f32_bf16 v165, v109, v109
  v_dot2c_f32_bf16 v162, s72, v110
  v_dot2c_f32_bf16 v163, s72, v111
  v_dot2c_f32_bf16 v162, s72, v112
  v_dot2c_f32_bf16 v163, s72, v113
  v_dot2c_f32_bf16 v164, v110, v110
  v_dot2c_f32_bf16 v165, v111, v111
  v_dot2c_f32_bf16 v164, v112, v112
  v_dot2c_f32_bf16 v165, v113, v113
  s_waitcnt lgkmcnt(8)
  v_dot2c_f32_bf16 v162, s72, v114
  v_dot2c_f32_bf16 v163, s72, v115
  s_add_u32 m0, s57, 43264
  s_nop 0
  buffer_load_dwordx4 v8, s[8:11], s68 offen offset:1792 lds
  v_dot2c_f32_bf16 v162, s72, v116
  v_dot2c_f32_bf16 v163, s72, v117
  v_dot2c_f32_bf16 v164, v114, v114
  v_dot2c_f32_bf16 v165, v115, v115
  v_dot2c_f32_bf16 v164, v116, v116
  v_dot2c_f32_bf16 v165, v117, v117
  v_dot2c_f32_bf16 v162, s72, v118
  v_dot2c_f32_bf16 v163, s72, v119
  v_dot2c_f32_bf16 v162, s72, v120
  v_dot2c_f32_bf16 v163, s72, v121
  v_dot2c_f32_bf16 v164, v118, v118
  v_dot2c_f32_bf16 v165, v119, v119
  v_dot2c_f32_bf16 v164, v120, v120
  v_dot2c_f32_bf16 v165, v121, v121
  s_waitcnt lgkmcnt(6)
  v_dot2c_f32_bf16 v166, s72, v142
  v_dot2c_f32_bf16 v167, s72, v143
  v_dot2c_f32_bf16 v166, s72, v144
  v_dot2c_f32_bf16 v167, s72, v145
  s_waitcnt vmcnt(4)
  s_barrier
  v_mfma_f32_32x32x16_bf16 v[146:161], v[142:145], v[106:109], v[146:161]
  ds_read_b64_tr_b16 v[138:139], v21 offset:24576
  ds_read_b64_tr_b16 v[140:141], v21 offset:24832
  s_waitcnt lgkmcnt(2)
  v_mfma_f32_32x32x16_bf16 v[146:161], a[12:15], v[110:113], v[146:161]
  ds_read_b128 v[122:125], v17 offset:32768
  ds_read_b128 v[126:129], v18 offset:32768
  s_add_u32 m0, s57, 47360
  s_nop 0
  buffer_load_dwordx4 v8, s[8:11], s69 offen offset:1792 lds
  v_mfma_f32_32x32x16_bf16 v[146:161], a[16:19], v[114:117], v[146:161]
  ds_read_b128 v[130:133], v19 offset:32768
  ds_read_b128 v[134:137], v20 offset:32768
  v_mfma_f32_32x32x16_bf16 v[146:161], a[20:23], v[118:121], v[146:161]
  ds_read_b64_tr_b16 a[0:1], v22 offset:24576
  ds_read_b64_tr_b16 a[2:3], v22 offset:24832
  ds_read_b64_tr_b16 a[4:5], v23 offset:24576
  ds_read_b64_tr_b16 a[6:7], v23 offset:24832
  ds_read_b64_tr_b16 a[8:9], v24 offset:24576
  ds_read_b64_tr_b16 a[10:11], v24 offset:24832
  s_waitcnt lgkmcnt(10)
  v_dot2c_f32_bf16 v166, s72, v138
  v_dot2c_f32_bf16 v167, s72, v139
  v_dot2c_f32_bf16 v166, s72, v140
  v_dot2c_f32_bf16 v167, s72, v141
  s_waitcnt lgkmcnt(8)
  v_dot2c_f32_bf16 v162, s72, v122
  v_dot2c_f32_bf16 v163, s72, v123
  v_dot2c_f32_bf16 v162, s72, v124
  v_dot2c_f32_bf16 v163, s72, v125
  v_dot2c_f32_bf16 v164, v122, v122
  v_dot2c_f32_bf16 v165, v123, v123
  v_dot2c_f32_bf16 v164, v124, v124
  v_dot2c_f32_bf16 v165, v125, v125
  v_dot2c_f32_bf16 v162, s72, v126
  v_dot2c_f32_bf16 v163, s72, v127
  v_dot2c_f32_bf16 v162, s72, v128
  v_dot2c_f32_bf16 v163, s72, v129
  v_dot2c_f32_bf16 v164, v126, v126
  v_dot2c_f32_bf16 v165, v127, v127
  s_add_u32 m0, s57, 51456
  s_nop 0
  buffer_load_dwordx4 v8, s[8:11], s70 offen offset:1792 lds
  v_dot2c_f32_bf16 v164, v128, v128
  v_dot2c_f32_bf16 v165, v129, v129
  s_waitcnt lgkmcnt(6)
  v_dot2c_f32_bf16 v162, s72, v130
  v_dot2c_f32_bf16 v163, s72, v131
  v_dot2c_f32_bf16 v162, s72, v132
  v_dot2c_f32_bf16 v163, s72, v133
  v_dot2c_f32_bf16 v164, v130, v130
  v_dot2c_f32_bf16 v165, v131, v131
  v_dot2c_f32_bf16 v164, v132, v132
  v_dot2c_f32_bf16 v165, v133, v133
  v_dot2c_f32_bf16 v162, s72, v134
  v_dot2c_f32_bf16 v163, s72, v135
  v_dot2c_f32_bf16 v162, s72, v136
  v_dot2c_f32_bf16 v163, s72, v137
  v_dot2c_f32_bf16 v164, v134, v134
  v_dot2c_f32_bf16 v165, v135, v135
  v_dot2c_f32_bf16 v164, v136, v136
  v_dot2c_f32_bf16 v165, v137, v137
  v_mfma_f32_32x32x16_bf16 v[146:161], v[138:141], v[122:125], v[146:161]
  v_add_f32 v166, v166, v167
  s_lshl_b32 s51, s49, 2
  s_add_u32 s51, s51, 145408
  v_add_u32 v1, s51, v12
  ds_write_b32 v1, v166
  v_add_f32 v162, v162, v163
  s_waitcnt lgkmcnt(1)
  v_mfma_f32_32x32x16_bf16 v[146:161], a[0:3], v[126:129], v[146:161]
  v_add_f32 v164, v164, v165
  v_add_u32 v2, 145408, v12
  v_subrev_u32 v21, 114688, v21
  v_subrev_u32 v22, 114688, v22
  v_subrev_u32 v23, 114688, v23
  v_subrev_u32 v24, 114688, v24
  v_mfma_f32_32x32x16_bf16 v[146:161], a[4:7], v[130:133], v[146:161]
  s_waitcnt vmcnt(5)
  s_waitcnt lgkmcnt(0)
  s_barrier
  ds_read_b128 v[218:221], v2
  v_permlane32_swap_b32 v162, v164
  v_add_f32 v162, v162, v164
  v_mfma_f32_32x32x16_bf16 v[146:161], a[8:11], v[134:137], v[146:161]
  v_mul_f32 v162, 0x3b124925, v162
  v_mov_b32 v164, v162
  s_nop 1
  v_permlane32_swap_b32 v162, v164
  v_fma_f32 v165, -v162, v162, v164
  v_sub_f32 v163, 0, v162
  v_max_f32 v165, 0, v165
  v_add_f32 v165, 0x3727c5ac, v165
  v_rsq_f32 v176, v165
  s_waitcnt lgkmcnt(0)
  v_add_f32 v218, v218, v219
  v_add_f32 v220, v220, v221
  v_add_f32 v218, v218, v220
  v_mul_f32 v165, v165, v176
  v_mov_b32 v219, v218
  s_nop 1
  v_permlane32_swap_b32 v218, v219
  v_add_f32 v218, v218, v219
  v_cvt_pk_bf16_f32 v168, v163, v163
  v_lshlrev_b32 v1, 16, v168
  v_sub_f32 v1, v163, v1
  v_cvt_pk_bf16_f32 v169, v1, v165
  v_cndmask_b32 v168, v168, 0, s[46:47]
  v_cndmask_b32 v169, v169, 0, s[46:47]
  v_cvt_pk_bf16_f32 v173, v218, 0
  v_lshlrev_b32 v2, 16, v173
  v_sub_f32 v2, v218, v2
  v_cvt_pk_bf16_f32 v172, v218, v2
  v_cndmask_b32 v172, v172, 0, s[46:47]
  v_cndmask_b32 v173, v173, 0, s[46:47]
  s_nop 1
  v_mfma_f32_32x32x16_bf16 v[146:161], v[172:175], v[168:171], v[146:161]
  ds_read_b64_tr_b16 a[24:25], v21 offset:32768
  ds_read_b64_tr_b16 a[26:27], v21 offset:33024
  ds_read_b64_tr_b16 a[28:29], v22 offset:32768
  ds_read_b64_tr_b16 a[30:31], v22 offset:33024
  v_mul_f32 v177, 0x3fb8aa3b, v176
  s_add_u32 m0, s57, 55552
  s_nop 0
  buffer_load_dwordx4 v8, s[8:11], s71 offen offset:1792 lds
  s_nop 3
  v_cvt_pk_bf16_f32 v234, v146, v147
  v_cvt_pk_bf16_f32 v235, v148, v149
  v_cvt_pk_bf16_f32 v236, v150, v151
  v_cvt_pk_bf16_f32 v237, v152, v153
  v_cvt_pk_bf16_f32 v238, v154, v155
  v_cvt_pk_bf16_f32 v239, v156, v157
  v_cvt_pk_bf16_f32 v240, v158, v159
  v_cvt_pk_bf16_f32 v241, v160, v161
  v_mfma_f32_32x32x16_bf16 v[202:217], v[178:181], v[234:237], 0
  ds_read_b64_tr_b16 a[32:33], v23 offset:32768
  ds_read_b64_tr_b16 a[34:35], v23 offset:33024
  v_mfma_f32_32x32x16_bf16 v[202:217], v[182:185], v[238:241], v[202:217]
  ds_read_b64_tr_b16 a[36:37], v24 offset:32768
  ds_read_b64_tr_b16 a[38:39], v24 offset:33024
  v_accvgpr_read_b32 v1, a248
  v_xor_b32 v2, 16, v1
  v_xor_b32 v4, 32, v1
  s_mov_b32 exec_hi, 0
  ds_write_b16_d16_hi v1, v169
  ds_write_b16 v2, v168 offset:256
  ds_write_b16 v4, v169 offset:512
  s_mov_b32 exec_hi, -1
  v_accvgpr_read_b32 v1, a242
  s_waitcnt vmcnt(4)
  s_barrier
  ds_read_b64_tr_b16 a[40:41], v21 offset:36864
  ds_read_b64_tr_b16 a[42:43], v21 offset:37120
  v_cndmask_b32 v214, v214, v202, s[46:47]
  v_cndmask_b32 v215, v215, v202, s[46:47]
  v_cndmask_b32 v216, v216, v202, s[46:47]
  v_cndmask_b32 v217, v217, v202, s[46:47]
  v_max3_f32 v4, v202, v203, v204
  v_min3_f32 v5, v202, v203, v204
  v_max3_f32 v4, v4, v205, v206
  v_min3_f32 v5, v5, v205, v206
  ds_read_b64_tr_b16 a[44:45], v22 offset:36864
  ds_read_b64_tr_b16 a[46:47], v22 offset:37120
  v_max3_f32 v4, v4, v207, v208
  v_min3_f32 v5, v5, v207, v208
  v_max3_f32 v4, v4, v209, v210
  v_min3_f32 v5, v5, v209, v210
  v_max3_f32 v4, v4, v211, v212
  v_min3_f32 v5, v5, v211, v212
  v_max3_f32 v4, v4, v213, v214
  v_min3_f32 v5, v5, v213, v214
  ds_read_b64_tr_b16 a[48:49], v23 offset:36864
  ds_read_b64_tr_b16 a[50:51], v23 offset:37120
  v_max3_f32 v4, v4, v215, v216
  v_min3_f32 v5, v5, v215, v216
  v_max_f32 v4, v4, v217
  v_min_f32 v5, v5, v217
  v_mov_b32 v6, v4
  v_mov_b32 v7, v5
  s_nop 0
  v_permlane32_swap_b32 v4, v6
  ds_read_b64_tr_b16 a[52:53], v24 offset:36864
  ds_read_b64_tr_b16 a[54:55], v24 offset:37120
  v_permlane32_swap_b32 v5, v7
  v_max_f32 v4, v4, v6
  v_min_f32 v5, v5, v7
  v_mul_f32 v4, v4, v177
  v_mul_f32 v5, v5, v177
  v_fma_f32 v218, -v202, v177, v5
  v_fma_f32 v202, v202, v177, -v4
  v_fma_f32 v219, -v203, v177, v5
  s_waitcnt lgkmcnt(11)
  v_mfma_f32_32x32x16_bf16 v[186:201], v[26:29], a[24:27], 0
  ds_read_b64_tr_b16 a[56:57], v21 offset:40960
  ds_read_b64_tr_b16 a[58:59], v21 offset:41216
  v_fma_f32 v203, v203, v177, -v4
  v_exp_f32 v218, v218
  v_exp_f32 v202, v202
  v_exp_f32 v219, v219
  v_mfma_f32_32x32x16_bf16 v[186:201], v[30:33], a[28:31], v[186:201]
  ds_read_b64_tr_b16 a[60:61], v22 offset:40960
  ds_read_b64_tr_b16 a[62:63], v22 offset:41216
  s_add_u32 m0, s57, 0
  s_nop 0
  buffer_load_dwordx4 v8, s[8:11], s65 offen lds
  v_exp_f32 v203, v203
  v_fma_f32 v220, -v204, v177, v5
  v_fma_f32 v204, v204, v177, -v4
  v_fma_f32 v221, -v205, v177, v5
  v_fma_f32 v205, v205, v177, -v4
  v_exp_f32 v220, v220
  v_mfma_f32_32x32x16_bf16 v[186:201], v[34:37], a[32:35], v[186:201]
  ds_read_b64_tr_b16 a[64:65], v23 offset:40960
  ds_read_b64_tr_b16 a[66:67], v23 offset:41216
  v_exp_f32 v204, v204
  v_exp_f32 v221, v221
  v_exp_f32 v205, v205
  v_add_f32 v9, v202, v203
  v_add_f32 v10, v218, v219
  v_mfma_f32_32x32x16_bf16 v[186:201], v[38:41], a[36:39], v[186:201]
  ds_read_b64_tr_b16 a[68:69], v24 offset:40960
  ds_read_b64_tr_b16 a[70:71], v24 offset:41216
  v_fma_f32 v222, -v206, v177, v5
  v_fma_f32 v206, v206, v177, -v4
  v_fma_f32 v223, -v207, v177, v5
  v_fma_f32 v207, v207, v177, -v4
  v_exp_f32 v222, v222
  v_exp_f32 v206, v206
  s_waitcnt vmcnt(3)
  s_barrier
  s_waitcnt lgkmcnt(8)
  v_mfma_f32_32x32x16_bf16 v[186:201], v[42:45], a[40:43], v[186:201]
  ds_read_b64_tr_b16 a[72:73], v21 offset:45056
  ds_read_b64_tr_b16 a[74:75], v21 offset:45312
  v_exp_f32 v223, v223
  v_exp_f32 v207, v207
  v_add_f32 v9, v9, v204
  v_add_f32 v10, v10, v220
  v_add_f32 v9, v9, v205
  v_mfma_f32_32x32x16_bf16 v[186:201], v[46:49], a[44:47], v[186:201]
  ds_read_b64_tr_b16 a[76:77], v22 offset:45056
  ds_read_b64_tr_b16 a[78:79], v22 offset:45312
  v_add_f32 v10, v10, v221
  v_fma_f32 v224, -v208, v177, v5
  v_fma_f32 v208, v208, v177, -v4
  v_fma_f32 v225, -v209, v177, v5
  v_fma_f32 v209, v209, v177, -v4
  v_exp_f32 v224, v224
  v_mfma_f32_32x32x16_bf16 v[186:201], v[50:53], a[48:51], v[186:201]
  ds_read_b64_tr_b16 a[80:81], v23 offset:45056
  ds_read_b64_tr_b16 a[82:83], v23 offset:45312
  v_exp_f32 v208, v208
  v_exp_f32 v225, v225
  v_exp_f32 v209, v209
  v_add_f32 v9, v9, v206
  v_add_f32 v10, v10, v222
  v_mfma_f32_32x32x16_bf16 v[186:201], v[54:57], a[52:55], v[186:201]
  ds_read_b64_tr_b16 a[84:85], v24 offset:45056
  ds_read_b64_tr_b16 a[86:87], v24 offset:45312
  s_add_u32 m0, s57, 4096
  s_nop 0
  buffer_load_dwordx4 v8, s[8:11], s66 offen lds
  v_add_f32 v9, v9, v207
  v_add_f32 v10, v10, v223
  v_fma_f32 v226, -v210, v177, v5
  v_fma_f32 v210, v210, v177, -v4
  v_fma_f32 v227, -v211, v177, v5
  v_fma_f32 v211, v211, v177, -v4
  v_exp_f32 v226, v226
  s_waitcnt lgkmcnt(8)
  v_mfma_f32_32x32x16_bf16 v[186:201], v[58:61], a[56:59], v[186:201]
  ds_read_b64_tr_b16 a[88:89], v21 offset:49152
  ds_read_b64_tr_b16 a[90:91], v21 offset:49408
  v_exp_f32 v210, v210
  v_exp_f32 v227, v227
  v_exp_f32 v211, v211
  v_add_f32 v9, v9, v208
  v_mfma_f32_32x32x16_bf16 v[186:201], v[62:65], a[60:63], v[186:201]
  ds_read_b64_tr_b16 a[92:93], v22 offset:49152
  ds_read_b64_tr_b16 a[94:95], v22 offset:49408
  v_add_f32 v10, v10, v224
  v_add_f32 v9, v9, v209
  v_add_f32 v10, v10, v225
  v_fma_f32 v228, -v212, v177, v5
  v_fma_f32 v212, v212, v177, -v4
  v_fma_f32 v229, -v213, v177, v5
  v_fma_f32 v213, v213, v177, -v4
  v_exp_f32 v228, v228
  v_mfma_f32_32x32x16_bf16 v[186:201], v[66:69], a[64:67], v[186:201]
  ds_read_b64_tr_b16 a[96:97], v23 offset:49152
  ds_read_b64_tr_b16 a[98:99], v23 offset:49408
  v_exp_f32 v212, v212
  v_exp_f32 v229, v229
  v_exp_f32 v213, v213
  v_mfma_f32_32x32x16_bf16 v[186:201], v[70:73], a[68:71], v[186:201]
  ds_read_b64_tr_b16 a[100:101], v24 offset:49152
  ds_read_b64_tr_b16 a[102:103], v24 offset:49408
  v_add_f32 v9, v9, v210
  v_add_f32 v10, v10, v226
  v_add_f32 v9, v9, v211
  v_add_f32 v10, v10, v227
  v_fma_f32 v230, -v214, v177, v5
  v_fma_f32 v214, v214, v177, -v4
  v_fma_f32 v231, -v215, v177, v5
  v_fma_f32 v215, v215, v177, -v4
  s_waitcnt vmcnt(2)
  s_barrier
  s_waitcnt lgkmcnt(8)
  v_mfma_f32_32x32x16_bf16 v[186:201], v[74:77], a[72:75], v[186:201]
  ds_read_b64_tr_b16 a[104:105], v21 offset:53248
  ds_read_b64_tr_b16 a[106:107], v21 offset:53504
  v_exp_f32 v230, v230
  v_exp_f32 v214, v214
  v_exp_f32 v231, v231
  v_exp_f32 v215, v215
  v_mfma_f32_32x32x16_bf16 v[186:201], v[78:81], a[76:79], v[186:201]
  ds_read_b64_tr_b16 a[108:109], v22 offset:53248
  ds_read_b64_tr_b16 a[110:111], v22 offset:53504
  s_mov_b32 m0, 149504
  v_and_b32 v147, 1, v3
  v_lshlrev_b32 v147, 7, v147
  v_lshrrev_b32 v146, 1, v3
  v_lshl_add_u32 v147, v146, 16, v147
  s_mov_b64 exec, 0xff
  buffer_load_dword v147, s[36:39], s83 offen sc1 lds
  s_mov_b64 exec, -1
  s_lshl_b32 s51, %1, 2
  s_add_u32 s51, s51, s49
  v_lshlrev_b32 v146, 7, v3
  s_mul_i32 s52, s51, 512
  s_mov_b64 exec, 0xf
  buffer_load_dword v146, s[16:19], s52 offen lds
  s_mul_i32 s52, s51, 1664
  s_mov_b64 exec, 0x1fff
  buffer_load_dword v146, s[20:23], s52 offen lds
  buffer_load_dword v146, s[24:27], s52 offen lds
  s_mov_b64 exec, -1
  v_add_f32 v9, v9, v212
  v_add_f32 v10, v10, v228
  v_add_f32 v9, v9, v213
  v_add_f32 v10, v10, v229
  v_fma_f32 v232, -v216, v177, v5
  v_fma_f32 v216, v216, v177, -v4
  v_fma_f32 v233, -v217, v177, v5
  v_mfma_f32_32x32x16_bf16 v[186:201], v[82:85], a[80:83], v[186:201]
  ds_read_b64_tr_b16 a[112:113], v23 offset:53248
  ds_read_b64_tr_b16 a[114:115], v23 offset:53504
  v_fma_f32 v217, v217, v177, -v4
  v_exp_f32 v232, v232
  v_exp_f32 v216, v216
  v_exp_f32 v233, v233
  v_exp_f32 v217, v217
  v_mfma_f32_32x32x16_bf16 v[186:201], v[86:89], a[84:87], v[186:201]
  ds_read_b64_tr_b16 a[116:117], v24 offset:53248
  ds_read_b64_tr_b16 a[118:119], v24 offset:53504
  v_add_f32 v6, v214, v215
  v_add_f32 v7, v230, v231
  v_add_f32 v4, v216, v217
  v_add_f32 v5, v232, v233
  v_add_f32 v6, v6, v4
  v_add_f32 v7, v7, v5
  s_waitcnt lgkmcnt(8)
  v_mfma_f32_32x32x16_bf16 v[186:201], v[90:93], a[88:91], v[186:201]
  ds_read_b64_tr_b16 a[120:121], v21 offset:57344
  ds_read_b64_tr_b16 a[122:123], v21 offset:57600
  v_cndmask_b32 v6, v6, 0, s[46:47]
  v_cndmask_b32 v7, v7, 0, s[46:47]
  v_add_f32 v9, v9, v6
  v_add_f32 v10, v10, v7
  v_mov_b32 v6, v9
  v_mov_b32 v7, v10
  s_nop 0
  v_permlane32_swap_b32 v9, v6
  v_permlane32_swap_b32 v10, v7
  v_mfma_f32_32x32x16_bf16 v[186:201], v[94:97], a[92:95], v[186:201]
  ds_read_b64_tr_b16 a[124:125], v22 offset:57344
  ds_read_b64_tr_b16 a[126:127], v22 offset:57600
  v_add_f32 v9, v9, v6
  v_add_f32 v10, v10, v7
  v_rcp_f32 v9, v9
  v_rcp_f32 v10, v10
  v_mul_f32 v9, v9, v176
  v_mul_f32 v10, v10, v176
  v_mfma_f32_32x32x16_bf16 v[186:201], v[98:101], a[96:99], v[186:201]
  ds_read_b64_tr_b16 a[128:129], v23 offset:57344
  ds_read_b64_tr_b16 a[130:131], v23 offset:57600
  v_cndmask_b32 v6, v9, 0, s[46:47]
  v_cndmask_b32 v7, v10, 0, s[46:47]
  v_mul_f32 v202, v202, v9
  v_mul_f32 v218, v218, v10
  v_mul_f32 v203, v203, v9
  v_mul_f32 v219, v219, v10
  v_cvt_pk_bf16_f32 v234, v202, v203
  v_mfma_f32_32x32x16_bf16 v[186:201], v[102:105], a[100:103], v[186:201]
  ds_read_b64_tr_b16 a[132:133], v24 offset:57344
  ds_read_b64_tr_b16 a[134:135], v24 offset:57600
  v_cvt_pk_bf16_f32 v242, v218, v219
  v_mul_f32 v204, v204, v9
  v_mul_f32 v220, v220, v10
  v_mul_f32 v205, v205, v9
  v_mul_f32 v221, v221, v10
  v_cvt_pk_bf16_f32 v235, v204, v205
  v_cvt_pk_bf16_f32 v243, v220, v221
  v_mul_f32 v206, v206, v9
  s_waitcnt lgkmcnt(8)
  v_mfma_f32_32x32x16_bf16 v[186:201], v[106:109], a[104:107], v[186:201]
  ds_write_b64 v1, v[234:235] offset:0
  ds_write_b64 v1, v[242:243] offset:64
  v_mfma_f32_32x32x16_bf16 v[186:201], v[110:113], a[108:111], v[186:201]
  v_mul_f32 v222, v222, v10
  v_mul_f32 v207, v207, v9
  v_mul_f32 v223, v223, v10
  v_cvt_pk_bf16_f32 v236, v206, v207
  v_cvt_pk_bf16_f32 v244, v222, v223
  v_mul_f32 v208, v208, v9
  v_mul_f32 v224, v224, v10
  v_mul_f32 v209, v209, v9
  v_mul_f32 v225, v225, v10
  v_cvt_pk_bf16_f32 v237, v208, v209
  v_cvt_pk_bf16_f32 v245, v224, v225
  v_mfma_f32_32x32x16_bf16 v[186:201], v[114:117], a[112:115], v[186:201]
  ds_write_b64 v1, v[236:237] offset:16
  ds_write_b64 v1, v[244:245] offset:80
  v_mfma_f32_32x32x16_bf16 v[186:201], v[118:121], a[116:119], v[186:201]
  v_mul_f32 v210, v210, v9
  v_mul_f32 v226, v226, v10
  v_mul_f32 v211, v211, v9
  v_mul_f32 v227, v227, v10
  v_cvt_pk_bf16_f32 v238, v210, v211
  v_cvt_pk_bf16_f32 v246, v226, v227
  v_mul_f32 v212, v212, v9
  v_mul_f32 v228, v228, v10
  v_mul_f32 v213, v213, v9
  v_mul_f32 v229, v229, v10
  v_cvt_pk_bf16_f32 v239, v212, v213
  v_cvt_pk_bf16_f32 v247, v228, v229
  s_waitcnt lgkmcnt(4)
  v_mfma_f32_32x32x16_bf16 v[186:201], v[122:125], a[120:123], v[186:201]
  ds_write_b64 v1, v[238:239] offset:32
  ds_write_b64 v1, v[246:247] offset:96
  v_mfma_f32_32x32x16_bf16 v[186:201], v[126:129], a[124:127], v[186:201]
  v_mul_f32 v214, v214, v6
  v_mul_f32 v230, v230, v7
  v_mul_f32 v215, v215, v6
  v_mul_f32 v231, v231, v7
  v_cvt_pk_bf16_f32 v240, v214, v215
  v_cvt_pk_bf16_f32 v248, v230, v231
  v_mul_f32 v216, v216, v6
  v_mul_f32 v232, v232, v7
  v_mul_f32 v217, v217, v6
  v_mul_f32 v233, v233, v7
  v_cvt_pk_bf16_f32 v241, v216, v217
  v_cvt_pk_bf16_f32 v249, v232, v233
  v_mfma_f32_32x32x16_bf16 v[186:201], v[130:133], a[128:131], v[186:201]
  ds_write_b64 v1, v[240:241] offset:48
  ds_write_b64 v1, v[248:249] offset:112
  v_mfma_f32_32x32x16_bf16 v[186:201], v[134:137], a[132:135], v[186:201]
  v_accvgpr_read_b32 v4, a243
  v_accvgpr_read_b32 v6, a244
  v_accvgpr_read_b32 v7, a245
  v_accvgpr_read_b32 v9, a246
  s_waitcnt lgkmcnt(0)
  s_barrier
  ds_read_b64_tr_b16 v[218:219], v4 offset:0
  ds_read_b64_tr_b16 v[220:221], v4 offset:544
  ds_read_b64_tr_b16 v[222:223], v4 offset:4352
  ds_read_b64_tr_b16 v[224:225], v4 offset:4896
  ds_read_b64_tr_b16 v[226:227], v4 offset:8704
  ds_read_b64_tr_b16 v[228:229], v4 offset:9248
  ds_read_b64_tr_b16 v[230:231], v4 offset:13056
  ds_read_b64_tr_b16 v[232:233], v4 offset:13600
  v_xor_b32 v203, 16, v6
  v_xor_b32 v204, 32, v6
  v_xor_b32 v205, 48, v6
  v_xor_b32 v207, 64, v7
  v_xor_b32 v208, 128, v7
  v_xor_b32 v209, 192, v7
  s_mov_b32 s34, 0x8fffffff
  s_mov_b32 s35, 0x8fffffff
  v_cvt_pk_bf16_f32 v234, v186, v187
  v_cvt_pk_bf16_f32 v235, v188, v189
  v_cvt_pk_bf16_f32 v236, v190, v191
  v_cvt_pk_bf16_f32 v237, v192, v193
  v_cvt_pk_bf16_f32 v238, v194, v195
  v_cvt_pk_bf16_f32 v239, v196, v197
  v_cvt_pk_bf16_f32 v240, v198, v199
  v_cvt_pk_bf16_f32 v241, v200, v201
  s_mov_b64 exec, s[34:35]
  ds_write_b64 v6, v[234:235]
  ds_write_b64 v203, v[236:237]
  ds_write_b64 v204, v[238:239]
  ds_write_b64 v205, v[240:241]
  s_mov_b64 exec, -1
  s_waitcnt lgkmcnt(0)
  s_barrier
  ds_read_b128 v[146:149], v7 offset:0
  ds_read_b128 v[150:153], v7 offset:4096
  ds_read_b128 v[154:157], v207 offset:0
  ds_read_b128 v[158:161], v207 offset:4096
  ds_read_b128 v[186:189], v208 offset:0
  ds_read_b128 v[190:193], v208 offset:4096
  ds_read_b128 v[194:197], v209 offset:0
  ds_read_b128 v[198:201], v209 offset:4096
  v_mov_b32 v18, s44
  v_mov_b32 v19, s45
  v_lshlrev_b32 v11, 12, v3
  v_lshlrev_b32 v17, 3, v3
  s_mov_b32 s13, 0
  s_mov_b32 s15, 0
  s_cmp_eq_u32 s49, 3
  s_cselect_b32 s12, 0xffff, 0
  s_cmp_eq_u32 s49, 0
  s_cselect_b32 s14, 0xffff, 0
  v_bfrev_b32 v1, 2
  v_cndmask_b32 v11, v1, v11, s[12:13]
  v_cndmask_b32 v20, v1, v17, s[14:15]
  s_waitcnt lgkmcnt(6)
  v_mfma_f32_16x16x32_bf16 v[210:213], v[218:221], v[146:149], 0
  v_mfma_f32_16x16x32_bf16 v[214:217], v[218:221], v[150:153], 0
  s_waitcnt lgkmcnt(4)
  v_mfma_f32_16x16x32_bf16 v[210:213], v[222:225], v[154:157], v[210:213]
  v_mfma_f32_16x16x32_bf16 v[214:217], v[222:225], v[158:161], v[214:217]
  s_waitcnt lgkmcnt(2)
  v_mfma_f32_16x16x32_bf16 v[210:213], v[226:229], v[186:189], v[210:213]
  v_mfma_f32_16x16x32_bf16 v[214:217], v[226:229], v[190:193], v[214:217]
  s_waitcnt lgkmcnt(0)
  v_mfma_f32_16x16x32_bf16 v[210:213], v[230:233], v[194:197], v[210:213]
  v_mfma_f32_16x16x32_bf16 v[214:217], v[230:233], v[198:201], v[214:217]
  s_nop 6
  v_cvt_pk_bf16_f32 v234, v210, v211
  v_cvt_pk_bf16_f32 v235, v212, v213
  v_cvt_pk_bf16_f32 v236, v214, v215
  v_cvt_pk_bf16_f32 v237, v216, v217
  s_nop 0
  v_permlane32_swap_b32 v234, v236
  v_permlane32_swap_b32 v235, v237
  buffer_store_dwordx4 v[234:237], v9, s[36:39], s80 offen sc1
  s_add_u32 m0, s57, 8192
  s_mul_i32 s18, s18, s76
  buffer_load_dwordx4 v8, s[8:11], s67 offen lds
  ds_read_b64_tr_b16 v[202:203], v21 offset:32768
  s_mul_i32 s22, s22, s76
  s_mul_i32 s40, s49, 14336
  ds_read_b64_tr_b16 v[204:205], v21 offset:33024
  s_mul_i32 s41, s49, 7168
  s_lshl_b32 s42, s49, 13
  ds_read_b64_tr_b16 v[206:207], v21 offset:36864
  s_lshl_b32 s51, s78, 16
  s_add_u32 s43, s42, s51
  ds_read_b64_tr_b16 v[208:209], v21 offset:37120
  s_add_u32 s43, s43, 65536
  s_lshl_b32 s51, s75, 7
  ds_read_b64_tr_b16 v[210:211], v21 offset:40960
  s_mul_i32 s55, s49, 100352
  s_add_u32 s55, s55, s51
  ds_read_b64_tr_b16 v[212:213], v21 offset:41216
  s_mul_i32 s59, s49, 401408
  s_add_u32 s59, s59, s51
  ds_read_b64_tr_b16 v[214:215], v21 offset:45056
  s_add_u32 m0, s57, 12288
  s_lshl_b32 s51, s73, 8
  buffer_load_dwordx4 v8, s[8:11], s68 offen lds
  s_mul_i32 s58, s49, 100352
  ds_read_b64_tr_b16 v[216:217], v21 offset:45312
  s_add_u32 s58, s58, s51
  ds_read_b64_tr_b16 v[218:219], v21 offset:49152
  ds_read_b64_tr_b16 v[220:221], v21 offset:49408
  ds_read_b64_tr_b16 v[222:223], v21 offset:53248
  ds_read_b64_tr_b16 v[224:225], v21 offset:53504
  ds_read_b64_tr_b16 v[226:227], v21 offset:57344
  ds_read_b64_tr_b16 v[228:229], v21 offset:57600
  v_accvgpr_read_b32 v242, a250
  v_accvgpr_read_b32 v246, a249
  v_add_u32 v243, 6144, v242
  v_add_u32 v247, 13312, v246
  v_add_u32 v244, 12288, v242
  v_add_u32 v248, 26624, v246
  v_add_u32 v245, 18432, v242
  v_add_u32 v249, 39936, v246
  v_mov_b32 v5, 0
  v_mov_b32 v6, 0
  v_mov_b32 v7, 0
  v_mov_b32 v9, 0
  s_waitcnt lgkmcnt(12)
  v_dot2c_f32_bf16 v5, s72, v202
  v_dot2c_f32_bf16 v6, s72, v203
  v_dot2c_f32_bf16 v5, s72, v204
  v_dot2c_f32_bf16 v6, s72, v205
  s_waitcnt lgkmcnt(10)
  v_dot2c_f32_bf16 v5, s72, v206
  v_dot2c_f32_bf16 v6, s72, v207
  v_dot2c_f32_bf16 v5, s72, v208
  v_dot2c_f32_bf16 v6, s72, v209
  s_waitcnt lgkmcnt(8)
  v_dot2c_f32_bf16 v5, s72, v210
  v_dot2c_f32_bf16 v6, s72, v211
  v_dot2c_f32_bf16 v5, s72, v212
  v_dot2c_f32_bf16 v6, s72, v213
  s_waitcnt lgkmcnt(6)
  v_dot2c_f32_bf16 v5, s72, v214
  v_dot2c_f32_bf16 v6, s72, v215
  v_dot2c_f32_bf16 v5, s72, v216
  v_dot2c_f32_bf16 v6, s72, v217
  s_waitcnt lgkmcnt(4)
  v_dot2c_f32_bf16 v5, s72, v218
  v_dot2c_f32_bf16 v6, s72, v219
  v_dot2c_f32_bf16 v5, s72, v220
  v_dot2c_f32_bf16 v6, s72, v221
  s_waitcnt lgkmcnt(2)
  v_dot2c_f32_bf16 v5, s72, v222
  v_dot2c_f32_bf16 v6, s72, v223
  v_dot2c_f32_bf16 v5, s72, v224
  v_dot2c_f32_bf16 v6, s72, v225
  s_waitcnt lgkmcnt(0)
  v_dot2c_f32_bf16 v5, s72, v226
  v_dot2c_f32_bf16 v6, s72, v227
  v_dot2c_f32_bf16 v5, s72, v228
  v_dot2c_f32_bf16 v6, s72, v229
  s_waitcnt vmcnt(2)
  s_waitcnt lgkmcnt(0)
  s_barrier
  buffer_store_dwordx2 v[18:19], v11, s[36:39], s81 offen sc1
  s_add_u32 m0, s57, 16384
  ds_read_b64_tr_b16 v[138:139], v21 offset:0
  buffer_load_dwordx4 v8, s[8:11], s69 offen lds
  ds_read_b64_tr_b16 v[140:141], v21 offset:256
  ds_read_b64_tr_b16 a[136:137], v22 offset:0
  ds_read_b64_tr_b16 a[138:139], v22 offset:256
  ds_read_b64_tr_b16 a[140:141], v23 offset:0
  ds_read_b64_tr_b16 a[142:143], v23 offset:256
  s_add_u32 m0, s57, 20480
  ds_read_b64_tr_b16 a[144:145], v24 offset:0
  buffer_load_dwordx4 v8, s[8:11], s70 offen lds
  ds_read_b64_tr_b16 a[146:147], v24 offset:256
  v_lshlrev_b32 v250, 4, v13
  v_lshl_add_u32 v250, v14, 12, v250
  s_waitcnt lgkmcnt(0)
  v_mfma_f32_32x32x16_bf16 v[186:201], v[138:141], v[26:29], 0
  ds_read_b64_tr_b16 v[142:143], v21 offset:4096
  ds_read_b64_tr_b16 v[144:145], v21 offset:4352
  v_mfma_f32_32x32x16_bf16 v[186:201], a[136:139], v[30:33], v[186:201]
  ds_read_b64_tr_b16 a[148:149], v22 offset:4096
  ds_read_b64_tr_b16 a[150:151], v22 offset:4352
  v_mfma_f32_32x32x16_bf16 v[186:201], a[140:143], v[34:37], v[186:201]
  s_add_u32 m0, s57, 24576
  ds_read_b64_tr_b16 a[152:153], v23 offset:4096
  buffer_load_dwordx4 v8, s[8:11], s71 offen lds
  ds_read_b64_tr_b16 a[154:155], v23 offset:4352
  v_mfma_f32_32x32x16_bf16 v[186:201], a[144:147], v[38:41], v[186:201]
  ds_read_b64_tr_b16 a[156:157], v24 offset:4096
  ds_read_b64_tr_b16 a[158:159], v24 offset:4352
  v_dot2c_f32_bf16 v7, s72, v138
  v_dot2c_f32_bf16 v9, s72, v139
  v_dot2c_f32_bf16 v7, s72, v140
  v_dot2c_f32_bf16 v9, s72, v141
  s_waitcnt vmcnt(4)
  s_barrier
  s_waitcnt lgkmcnt(0)
  v_mfma_f32_32x32x16_bf16 v[186:201], v[142:145], v[42:45], v[186:201]
  ds_read_b64_tr_b16 v[138:139], v21 offset:8192
  ds_read_b64_tr_b16 v[140:141], v21 offset:8448
  v_mfma_f32_32x32x16_bf16 v[186:201], a[148:151], v[46:49], v[186:201]
  ds_read_b64_tr_b16 a[160:161], v22 offset:8192
  ds_read_b64_tr_b16 a[162:163], v22 offset:8448
  v_mfma_f32_32x32x16_bf16 v[186:201], a[152:155], v[50:53], v[186:201]
  ds_read_b64_tr_b16 a[164:165], v23 offset:8192
  ds_read_b64_tr_b16 a[166:167], v23 offset:8448
  v_mfma_f32_32x32x16_bf16 v[186:201], a[156:159], v[54:57], v[186:201]
  ds_read_b64_tr_b16 a[168:169], v24 offset:8192
  ds_read_b64_tr_b16 a[170:171], v24 offset:8448
  v_dot2c_f32_bf16 v7, s72, v142
  v_dot2c_f32_bf16 v9, s72, v143
  v_dot2c_f32_bf16 v7, s72, v144
  v_dot2c_f32_bf16 v9, s72, v145
  s_waitcnt lgkmcnt(0)
  v_mfma_f32_32x32x16_bf16 v[186:201], v[138:141], v[58:61], v[186:201]
  ds_read_b64_tr_b16 v[142:143], v21 offset:12288
  buffer_load_dwordx2 v[162:163], v20, s[36:39], s83 offen sc1
  ds_read_b64_tr_b16 v[144:145], v21 offset:12544
  v_mfma_f32_32x32x16_bf16 v[186:201], a[160:163], v[62:65], v[186:201]
  ds_read_b64_tr_b16 a[172:173], v22 offset:12288
  ds_read_b64_tr_b16 a[174:175], v22 offset:12544
  v_mfma_f32_32x32x16_bf16 v[186:201], a[164:167], v[66:69], v[186:201]
  ds_read_b64_tr_b16 a[176:177], v23 offset:12288
  ds_read_b64_tr_b16 a[178:179], v23 offset:12544
  v_mfma_f32_32x32x16_bf16 v[186:201], a[168:171], v[70:73], v[186:201]
  ds_read_b64_tr_b16 a[180:181], v24 offset:12288
  ds_read_b64_tr_b16 a[182:183], v24 offset:12544
  v_dot2c_f32_bf16 v7, s72, v138
  v_dot2c_f32_bf16 v9, s72, v139
  v_dot2c_f32_bf16 v7, s72, v140
  v_dot2c_f32_bf16 v9, s72, v141
  s_waitcnt vmcnt(2)
  s_barrier
  s_waitcnt lgkmcnt(0)
  v_mfma_f32_32x32x16_bf16 v[186:201], v[142:145], v[74:77], v[186:201]
  ds_read_b64_tr_b16 v[138:139], v21 offset:16384
  ds_read_b64_tr_b16 v[140:141], v21 offset:16640
  v_mfma_f32_32x32x16_bf16 v[186:201], a[172:175], v[78:81], v[186:201]
  ds_read_b64_tr_b16 a[184:185], v22 offset:16384
  ds_read_b64_tr_b16 a[186:187], v22 offset:16640
  v_mfma_f32_32x32x16_bf16 v[186:201], a[176:179], v[82:85], v[186:201]
  ds_read_b64_tr_b16 a[188:189], v23 offset:16384
  ds_read_b64_tr_b16 a[190:191], v23 offset:16640
  v_mfma_f32_32x32x16_bf16 v[186:201], a[180:183], v[86:89], v[186:201]
  ds_read_b64_tr_b16 a[192:193], v24 offset:16384
  ds_read_b64_tr_b16 a[194:195], v24 offset:16640
  v_dot2c_f32_bf16 v7, s72, v142
  v_dot2c_f32_bf16 v9, s72, v143
  v_dot2c_f32_bf16 v7, s72, v144
  v_dot2c_f32_bf16 v9, s72, v145
  s_waitcnt vmcnt(0)
  v_cmp_ne_u64 vcc, s[44:45], v[162:163]
  s_and_b64 vcc, s[14:15], vcc
  s_cbranch_vccz Lok0
  s_mov_b64 exec, s[14:15]
  s_mov_b32 s35, 1048576
Lpl0:
  buffer_load_dwordx2 v[162:163], v17, s[36:39], s83 offen sc1
  s_waitcnt vmcnt(0)
  v_cmp_ne_u64 vcc, s[44:45], v[162:163]
  s_and_b64 vcc, exec, vcc
  s_cbranch_vccz Lpx0
  s_sub_u32 s35, s35, 1
  s_cbranch_scc0 Lpl0
Lpx0:
  s_mov_b64 exec, -1
Lok0:
  s_add_u32 s83, s83, 65536
  s_barrier
  buffer_load_dwordx4 a[0:3], v250, s[36:39], s85 offen sc1
  s_waitcnt lgkmcnt(0)
  v_mfma_f32_32x32x16_bf16 v[186:201], v[138:141], v[90:93], v[186:201]
  ds_read_b64_tr_b16 v[142:143], v21 offset:20480
  ds_read_b64_tr_b16 v[144:145], v21 offset:20736
  v_mfma_f32_32x32x16_bf16 v[186:201], a[184:187], v[94:97], v[186:201]
  ds_read_b64_tr_b16 a[196:197], v22 offset:20480
  ds_read_b64_tr_b16 a[198:199], v22 offset:20736
  buffer_load_dwordx4 a[4:7], v250, s[36:39], s85 offen offset:512 sc1
  v_mfma_f32_32x32x16_bf16 v[186:201], a[188:191], v[98:101], v[186:201]
  ds_read_b64_tr_b16 a[200:201], v23 offset:20480
  ds_read_b64_tr_b16 a[202:203], v23 offset:20736
  v_mfma_f32_32x32x16_bf16 v[186:201], a[192:195], v[102:105], v[186:201]
  ds_read_b64_tr_b16 a[204:205], v24 offset:20480
  ds_read_b64_tr_b16 a[206:207], v24 offset:20736
  s_addk_i32 s85, 8192
  buffer_load_dwordx4 a[8:11], v250, s[36:39], s85 offen sc1
  v_dot2c_f32_bf16 v7, s72, v138
  v_dot2c_f32_bf16 v9, s72, v139
  v_dot2c_f32_bf16 v7, s72, v140
  v_dot2c_f32_bf16 v9, s72, v141
  s_barrier
  buffer_load_dwordx4 a[12:15], v250, s[36:39], s85 offen offset:512 sc1
  s_waitcnt lgkmcnt(0)
  v_mfma_f32_32x32x16_bf16 v[186:201], v[142:145], v[106:109], v[186:201]
  ds_read_b64_tr_b16 v[138:139], v21 offset:24576
  ds_read_b64_tr_b16 v[140:141], v21 offset:24832
  v_mfma_f32_32x32x16_bf16 v[186:201], a[196:199], v[110:113], v[186:201]
  ds_read_b64_tr_b16 a[208:209], v22 offset:24576
  ds_read_b64_tr_b16 a[210:211], v22 offset:24832
  s_addk_i32 s85, 8192
  buffer_load_dwordx4 a[16:19], v250, s[36:39], s85 offen sc1
  v_mfma_f32_32x32x16_bf16 v[186:201], a[200:203], v[114:117], v[186:201]
  ds_read_b64_tr_b16 a[212:213], v23 offset:24576
  ds_read_b64_tr_b16 a[214:215], v23 offset:24832
  v_mfma_f32_32x32x16_bf16 v[186:201], a[204:207], v[118:121], v[186:201]
  ds_read_b64_tr_b16 a[216:217], v24 offset:24576
  ds_read_b64_tr_b16 a[218:219], v24 offset:24832
  buffer_load_dwordx4 a[20:23], v250, s[36:39], s85 offen offset:512 sc1
  v_dot2c_f32_bf16 v7, s72, v142
  v_dot2c_f32_bf16 v9, s72, v143
  v_dot2c_f32_bf16 v7, s72, v144
  v_dot2c_f32_bf16 v9, s72, v145
  s_waitcnt lgkmcnt(0)
  v_dot2c_f32_bf16 v7, s72, v138
  v_dot2c_f32_bf16 v9, s72, v139
  v_dot2c_f32_bf16 v7, s72, v140
  v_dot2c_f32_bf16 v9, s72, v141
  s_lshl_b32 s51, s49, 2
  s_addk_i32 s85, 8192
  buffer_load_dwordx4 a[24:27], v250, s[36:39], s85 offen sc1
  s_add_u32 s51, s51, 145408
  v_add_u32 v1, s51, v12
  v_add_f32 v5, v5, v6
  v_add_f32 v7, v7, v9
  s_nop 1
  v_permlane32_swap_b32 v5, v7
  v_add_f32 v5, v5, v7
  ds_write_b32 v1, v5
  buffer_load_dwordx4 a[28:31], v250, s[36:39], s85 offen offset:512 sc1
  v_mfma_f32_32x32x16_bf16 v[186:201], v[138:141], v[122:125], v[186:201]
  v_add_u32 v1, 145408, v12
  s_waitcnt lgkmcnt(0)
  s_sub_u32 s85, s85, 24576
  s_xor_b32 s85, s85, 0x8000
  buffer_load_dwordx4 a[32:35], v250, s[36:39], s85 offen sc1
  s_barrier
  ds_read_b128 v[218:221], v1
  s_lshl_b32 s51, s49, 8
  v_mfma_f32_32x32x16_bf16 v[186:201], a[208:211], v[126:129], v[186:201]
  s_add_u32 s51, s51, 146432
  buffer_load_dwordx4 a[36:39], v250, s[36:39], s85 offen offset:512 sc1
  v_lshl_add_u32 v1, v3, 2, s51
  s_waitcnt lgkmcnt(0)
  v_add_f32 v218, v218, v219
  v_add_f32 v220, v220, v221
  v_add_f32 v218, v218, v220
  v_mfma_f32_32x32x16_bf16 v[186:201], a[212:215], v[130:133], v[186:201]
  ds_write_b32 v1, v218
  v_mov_b32 v219, v218
  s_nop 1
  v_permlane32_swap_b32 v218, v219
  v_cvt_pk_bf16_f32 v173, v219, 0
  v_lshlrev_b32 v2, 16, v173
  v_mfma_f32_32x32x16_bf16 v[186:201], a[216:219], v[134:137], v[186:201]
  s_addk_i32 s85, 8192
  buffer_load_dwordx4 a[40:43], v250, s[36:39], s85 offen sc1
  v_sub_f32 v2, v219, v2
  v_cvt_pk_bf16_f32 v172, v219, v2
  v_cndmask_b32 v172, v172, 0, s[46:47]
  v_cndmask_b32 v173, v173, 0, s[46:47]
  s_nop 1
  v_mfma_f32_32x32x16_bf16 v[186:201], v[172:175], v[168:171], v[186:201]
  v_lshl_add_u32 v1, v14, 4, s51
  ds_read_b128 v[146:149], v1 offset:0
  ds_read_b128 v[150:153], v1 offset:32
  ds_read_b128 v[154:157], v1 offset:64
  ds_read_b128 v[158:161], v1 offset:96
  buffer_load_dwordx4 a[44:47], v250, s[36:39], s85 offen offset:512 sc1
  v_accvgpr_read_b32 v26, a240
  v_accvgpr_read_b32 v27, a241
  s_nop 3
  v_cvt_pk_bf16_f32 v234, v186, v187
  v_cvt_pk_bf16_f32 v235, v188, v189
  v_cvt_pk_bf16_f32 v236, v190, v191
  v_cvt_pk_bf16_f32 v237, v192, v193
  v_cvt_pk_bf16_f32 v238, v194, v195
  v_cvt_pk_bf16_f32 v239, v196, v197
  v_cvt_pk_bf16_f32 v240, v198, v199
  v_cvt_pk_bf16_f32 v241, v200, v201
  v_mfma_f32_32x32x16_bf16 v[202:217], v[178:181], v[234:237], 0
  s_addk_i32 s85, 8192
  buffer_load_dwordx4 a[48:51], v250, s[36:39], s85 offen sc1
  v_mfma_f32_32x32x16_bf16 v[202:217], v[182:185], v[238:241], v[202:217]
  s_nop 11
  v_cndmask_b32 v214, v214, v202, s[46:47]
  v_cndmask_b32 v215, v215, v202, s[46:47]
  buffer_load_dwordx4 a[52:55], v250, s[36:39], s85 offen offset:512 sc1
  v_cndmask_b32 v216, v216, v202, s[46:47]
  v_cndmask_b32 v217, v217, v202, s[46:47]
  v_max3_f32 v4, v202, v203, v204
  v_min3_f32 v5, v202, v203, v204
  v_max3_f32 v4, v4, v205, v206
  v_min3_f32 v5, v5, v205, v206
  v_max3_f32 v4, v4, v207, v208
  v_min3_f32 v5, v5, v207, v208
  v_max3_f32 v4, v4, v209, v210
  v_min3_f32 v5, v5, v209, v210
  v_max3_f32 v4, v4, v211, v212
  v_min3_f32 v5, v5, v211, v212
  v_max3_f32 v4, v4, v213, v214
  v_min3_f32 v5, v5, v213, v214
  v_max3_f32 v4, v4, v215, v216
  v_min3_f32 v5, v5, v215, v216
  s_addk_i32 s85, 8192
  buffer_load_dwordx4 a[56:59], v250, s[36:39], s85 offen sc1
  v_max_f32 v4, v4, v217
  v_min_f32 v5, v5, v217
  v_mov_b32 v6, v4
  v_mov_b32 v7, v5
  s_nop 0
  v_permlane32_swap_b32 v4, v6
  v_permlane32_swap_b32 v5, v7
  v_max_f32 v4, v4, v6
  v_min_f32 v5, v5, v7
  v_mul_f32 v4, v4, v177
  v_mul_f32 v5, v5, v177
  v_fma_f32 v218, -v202, v177, v5
  v_fma_f32 v202, v202, v177, -v4
  v_fma_f32 v219, -v203, v177, v5
  v_fma_f32 v203, v203, v177, -v4
  v_exp_f32 v218, v218
  buffer_load_dwordx4 a[60:63], v250, s[36:39], s85 offen offset:512 sc1
  v_exp_f32 v202, v202
  v_exp_f32 v219, v219
  v_exp_f32 v203, v203
  v_fma_f32 v220, -v204, v177, v5
  v_fma_f32 v204, v204, v177, -v4
  v_fma_f32 v221, -v205, v177, v5
  v_fma_f32 v205, v205, v177, -v4
  v_exp_f32 v220, v220
  v_exp_f32 v204, v204
  v_exp_f32 v221, v221
  v_exp_f32 v205, v205
  v_add_f32 v9, v202, v203
  v_add_f32 v10, v218, v219
  v_fma_f32 v222, -v206, v177, v5
  v_fma_f32 v206, v206, v177, -v4
  v_fma_f32 v223, -v207, v177, v5
  v_fma_f32 v207, v207, v177, -v4
  v_exp_f32 v222, v222
  v_exp_f32 v206, v206
  v_exp_f32 v223, v223
  v_exp_f32 v207, v207
  v_add_f32 v9, v9, v204
  v_add_f32 v10, v10, v220
  v_add_f32 v9, v9, v205
  v_add_f32 v10, v10, v221
  v_fma_f32 v224, -v208, v177, v5
  v_fma_f32 v208, v208, v177, -v4
  v_fma_f32 v225, -v209, v177, v5
  v_fma_f32 v209, v209, v177, -v4
  v_exp_f32 v224, v224
  v_exp_f32 v208, v208
  v_exp_f32 v225, v225
  v_exp_f32 v209, v209
  v_add_f32 v9, v9, v206
  v_add_f32 v10, v10, v222
  v_add_f32 v9, v9, v207
  v_add_f32 v10, v10, v223
  v_fma_f32 v226, -v210, v177, v5
  v_fma_f32 v210, v210, v177, -v4
  v_fma_f32 v227, -v211, v177, v5
  v_fma_f32 v211, v211, v177, -v4
  s_waitcnt vmcnt(14)
  v_mfma_f32_16x16x32_bf16 v[138:141], a[252:255], a[0:3], 0
  v_exp_f32 v226, v226
  v_exp_f32 v210, v210
  v_exp_f32 v227, v227
  v_exp_f32 v211, v211
  v_add_f32 v9, v9, v208
  v_add_f32 v10, v10, v224
  v_mfma_f32_16x16x32_bf16 v[142:145], a[252:255], a[4:7], 0
  v_add_f32 v9, v9, v209
  v_add_f32 v10, v10, v225
  v_fma_f32 v228, -v212, v177, v5
  v_fma_f32 v212, v212, v177, -v4
  v_fma_f32 v229, -v213, v177, v5
  v_fma_f32 v213, v213, v177, -v4
  v_exp_f32 v228, v228
  v_exp_f32 v212, v212
  s_waitcnt vmcnt(12)
  v_mfma_f32_16x16x32_bf16 v[138:141], a[252:255], a[8:11], v[138:141]
  v_exp_f32 v229, v229
  v_exp_f32 v213, v213
  v_add_f32 v9, v9, v210
  v_add_f32 v10, v10, v226
  v_add_f32 v9, v9, v211
  v_add_f32 v10, v10, v227
  v_fma_f32 v230, -v214, v177, v5
  v_fma_f32 v214, v214, v177, -v4
  v_mfma_f32_16x16x32_bf16 v[142:145], a[252:255], a[12:15], v[142:145]
  v_fma_f32 v231, -v215, v177, v5
  v_fma_f32 v215, v215, v177, -v4
  v_exp_f32 v230, v230
  v_exp_f32 v214, v214
  v_exp_f32 v231, v231
  v_exp_f32 v215, v215
  s_waitcnt vmcnt(10)
  v_mfma_f32_16x16x32_bf16 v[138:141], a[252:255], a[16:19], v[138:141]
  v_add_f32 v9, v9, v212
  v_add_f32 v10, v10, v228
  v_add_f32 v9, v9, v213
  v_add_f32 v10, v10, v229
  v_fma_f32 v232, -v216, v177, v5
  v_fma_f32 v216, v216, v177, -v4
  v_fma_f32 v233, -v217, v177, v5
  v_fma_f32 v217, v217, v177, -v4
  v_exp_f32 v232, v232
  v_mfma_f32_16x16x32_bf16 v[142:145], a[252:255], a[20:23], v[142:145]
  v_exp_f32 v216, v216
  v_exp_f32 v233, v233
  v_exp_f32 v217, v217
  v_add_f32 v6, v214, v215
  v_add_f32 v7, v230, v231
  v_add_f32 v4, v216, v217
  v_add_f32 v5, v232, v233
  s_waitcnt vmcnt(8)
  v_mfma_f32_16x16x32_bf16 v[138:141], a[252:255], a[24:27], v[138:141]
  v_add_f32 v6, v6, v4
  v_add_f32 v7, v7, v5
  v_cndmask_b32 v6, v6, 0, s[46:47]
  v_cndmask_b32 v7, v7, 0, s[46:47]
  v_add_f32 v9, v9, v6
  v_add_f32 v10, v10, v7
  v_mov_b32 v6, v9
  v_mov_b32 v7, v10
  s_nop 0
  v_permlane32_swap_b32 v9, v6
  v_permlane32_swap_b32 v10, v7
  v_mfma_f32_16x16x32_bf16 v[142:145], a[252:255], a[28:31], v[142:145]
  v_add_f32 v9, v9, v6
  v_add_f32 v10, v10, v7
  v_rcp_f32 v9, v9
  v_rcp_f32 v10, v10
  v_cndmask_b32 v6, v9, 0, s[46:47]
  v_cndmask_b32 v7, v10, 0, s[46:47]
  v_mul_f32 v202, v202, v9
  v_mul_f32 v218, v218, v10
  s_waitcnt vmcnt(6)
  v_mfma_f32_16x16x32_bf16 v[138:141], a[252:255], a[32:35], v[138:141]
  v_mul_f32 v203, v203, v9
  v_mul_f32 v219, v219, v10
  v_cvt_pk_bf16_f32 v30, v202, v203
  v_cvt_pk_bf16_f32 v38, v218, v219
  v_mul_f32 v204, v204, v9
  v_mul_f32 v220, v220, v10
  v_mul_f32 v205, v205, v9
  v_mul_f32 v221, v221, v10
  v_cvt_pk_bf16_f32 v31, v204, v205
  v_cvt_pk_bf16_f32 v39, v220, v221
  v_mfma_f32_16x16x32_bf16 v[142:145], a[252:255], a[36:39], v[142:145]
  v_mul_f32 v206, v206, v9
  v_mul_f32 v222, v222, v10
  v_mul_f32 v207, v207, v9
  v_mul_f32 v223, v223, v10
  v_cvt_pk_bf16_f32 v32, v206, v207
  v_cvt_pk_bf16_f32 v40, v222, v223
  v_mul_f32 v208, v208, v9
  v_mul_f32 v224, v224, v10
  v_mul_f32 v209, v209, v9
  v_mul_f32 v225, v225, v10
  s_waitcnt vmcnt(4)
  v_mfma_f32_16x16x32_bf16 v[138:141], a[252:255], a[40:43], v[138:141]
  v_cvt_pk_bf16_f32 v33, v208, v209
  v_cvt_pk_bf16_f32 v41, v224, v225
  v_mul_f32 v210, v210, v9
  v_mul_f32 v226, v226, v10
  v_mul_f32 v211, v211, v9
  v_mul_f32 v227, v227, v10
  v_cvt_pk_bf16_f32 v34, v210, v211
  v_cvt_pk_bf16_f32 v42, v226, v227
  v_mul_f32 v212, v212, v9
  v_mul_f32 v228, v228, v10
  v_mfma_f32_16x16x32_bf16 v[142:145], a[252:255], a[44:47], v[142:145]
  v_mul_f32 v213, v213, v9
  v_mul_f32 v229, v229, v10
  v_cvt_pk_bf16_f32 v35, v212, v213
  v_cvt_pk_bf16_f32 v43, v228, v229
  v_mul_f32 v214, v214, v6
  v_mul_f32 v230, v230, v7
  v_mul_f32 v215, v215, v6
  v_mul_f32 v231, v231, v7
  v_cvt_pk_bf16_f32 v36, v214, v215
  v_cvt_pk_bf16_f32 v44, v230, v231
  v_mul_f32 v216, v216, v6
  v_mul_f32 v232, v232, v7
  v_mul_f32 v217, v217, v6
  v_mul_f32 v233, v233, v7
  v_cvt_pk_bf16_f32 v37, v216, v217
  v_cvt_pk_bf16_f32 v45, v232, v233
  v_accvgpr_read_b32 v4, a247
  v_add_u32 v5, 147456, v12
  v_lshlrev_b32 v11, 8, v3
  v_bfrev_b32 v6, 2
  v_cndmask_b32 v11, v6, v11, s[12:13]
  s_lshl_b32 s52, s49, 11
  s_add_u32 m0, s52, 155648
  s_nop 0
  buffer_load_dwordx4 v26, s[4:7], s86 offen lds
  buffer_load_dwordx4 v27, s[4:7], s86 offen offset:1024 lds
  s_waitcnt vmcnt(4)
  v_mfma_f32_16x16x32_bf16 v[138:141], a[252:255], a[48:51], v[138:141]
  v_mfma_f32_16x16x32_bf16 v[142:145], a[252:255], a[52:55], v[142:145]
  s_waitcnt vmcnt(2)
  v_mfma_f32_16x16x32_bf16 v[138:141], a[252:255], a[56:59], v[138:141]
  v_mfma_f32_16x16x32_bf16 v[142:145], a[252:255], a[60:63], v[142:145]
  s_nop 6
  v_cvt_pk_bf16_f32 v234, v138, v139
  v_cvt_pk_bf16_f32 v235, v140, v141
  v_cvt_pk_bf16_f32 v236, v142, v143
  v_cvt_pk_bf16_f32 v237, v144, v145
  ds_write_b64 v4, v[234:235]
  ds_write_b64 v4, v[236:237] offset:512
  s_waitcnt lgkmcnt(0)
  s_barrier
  ds_read_b128 v[218:221], v5 offset:0
  ds_read_b128 v[222:225], v5 offset:1024
  ds_read_b128 v[226:229], v5 offset:2048
  ds_read_b128 v[230:233], v5 offset:3072
  s_waitcnt lgkmcnt(3)
  v_mfma_f32_32x32x16_bf16 v[202:217], v[218:221], v[30:33], 0
  s_add_u32 m0, s40, 86016
  s_add_u32 s54, s55, 0
  buffer_load_dwordx4 v242, s[16:19], s54 offen lds
  s_waitcnt lgkmcnt(2)
  v_mfma_f32_32x32x16_bf16 v[202:217], v[222:225], v[34:37], v[202:217]
  s_waitcnt lgkmcnt(1)
  v_mfma_f32_32x32x16_bf16 v[202:217], v[226:229], v[38:41], v[202:217]
  buffer_load_dwordx4 v243, s[16:19], s54 offen offset:1024 lds
  s_waitcnt lgkmcnt(0)
  v_mfma_f32_32x32x16_bf16 v[202:217], v[230:233], v[42:45], v[202:217]
  s_nop 11
  v_add_f32 v1, v215, v216
  v_mov_b32 v2, v214
  buffer_load_dwordx4 v244, s[16:19], s54 offen offset:2048 lds
  v_mov_b32 v5, v1
  v_mov_b32 v6, v2
  s_nop 0
  v_permlane32_swap_b32 v5, v1
  v_permlane32_swap_b32 v6, v2
  v_add_f32 v2, 0x358637bd, v2
  v_rcp_f32 v2, v2
  v_fmac_f32 v202, v146, v1
  v_fmac_f32 v203, v147, v1
  v_fmac_f32 v204, v148, v1
  v_fmac_f32 v205, v149, v1
  v_fmac_f32 v206, v150, v1
  v_fmac_f32 v207, v151, v1
  v_fmac_f32 v208, v152, v1
  buffer_load_dwordx4 v245, s[16:19], s54 offen offset:3072 lds
  v_fmac_f32 v209, v153, v1
  v_mul_f32 v202, v202, v2
  v_mul_f32 v203, v203, v2
  v_cvt_pk_bf16_f32 v234, v202, v203
  v_mul_f32 v204, v204, v2
  v_mul_f32 v205, v205, v2
  v_cvt_pk_bf16_f32 v235, v204, v205
  v_mul_f32 v206, v206, v2
  v_mul_f32 v207, v207, v2
  v_cvt_pk_bf16_f32 v236, v206, v207
  v_mul_f32 v208, v208, v2
  v_mul_f32 v209, v209, v2
  v_cvt_pk_bf16_f32 v237, v208, v209
  buffer_store_dwordx4 v[234:237], v12, s[36:39], s84 offen sc1
  v_fmac_f32 v210, v154, v1
  v_fmac_f32 v211, v155, v1
  v_fmac_f32 v212, v156, v1
  v_fmac_f32 v213, v157, v1
  v_fmac_f32 v214, v158, v1
  v_fmac_f32 v215, v159, v1
  v_fmac_f32 v216, v160, v1
  v_fmac_f32 v217, v161, v1
  v_cndmask_b32 v1, v2, 0, s[46:47]
  v_mul_f32 v210, v210, v2
  v_mul_f32 v211, v211, v2
  v_cvt_pk_bf16_f32 v238, v210, v211
  v_mul_f32 v212, v212, v2
  v_mul_f32 v213, v213, v2
  v_cvt_pk_bf16_f32 v239, v212, v213
  v_mul_f32 v214, v214, v1
  v_mul_f32 v215, v215, v1
  v_cvt_pk_bf16_f32 v240, v214, v215
  v_mul_f32 v216, v216, v1
  v_mul_f32 v217, v217, v1
  v_cvt_pk_bf16_f32 v241, v216, v217
  buffer_store_dwordx4 v[238:241], v12, s[36:39], s84 offen offset:1024 sc1
  s_add_u32 m0, s40, 90112
  s_add_u32 s54, s55, 28672
  buffer_load_dwordx4 v242, s[16:19], s54 offen lds
  v_lshl_add_u32 v8, v25, 7, v16
  v_and_b32 v1, 2, v15
  v_lshl_add_u32 v8, v1, 5, v8
  s_mul_i32 s51, s78, 28672
  s_add_u32 s51, s51, 86016
  s_lshl_b32 s52, s78, 6
  v_xor_b32 v1, s52, v8
  v_add_u32 v22, s51, v1
  v_xor_b32 v23, 64, v22
  v_xor_b32 v1, s49, v15
  v_lshlrev_b32 v1, 6, v1
  v_lshl_add_u32 v1, v25, 8, v1
  v_add_u32 v21, v1, v16
  v_mov_b32 v138, s72
  v_mov_b32 v139, s72
  v_mov_b32 v140, s72
  v_mov_b32 v141, s72
  s_lshl_b32 s51, s33, 2
  buffer_load_dwordx4 v243, s[16:19], s54 offen offset:1024 lds
  s_add_u32 s53, s51, s79
  s_lshl_b32 s51, s53, 15
  s_lshl_b32 s52, s78, 14
  s_add_u32 s51, s51, s52
  buffer_load_dwordx4 v244, s[16:19], s54 offen offset:2048 lds
  s_add_u32 s62, s51, 2097152
  s_lshl_b32 s51, s49, 11
  s_add_u32 s51, s51, 147456
  v_add_u32 v4, s51, v12
  v_xor_b32 v5, 4096, v4
  s_mul_i32 s51, s53, 14
  s_lshl_b32 s52, s75, 1
  s_add_u32 s51, s51, s52
  s_add_u32 s51, s51, s78
  s_lshl_b32 s60, s51, 11
  buffer_load_dwordx4 v245, s[16:19], s54 offen offset:3072 lds
  s_add_u32 s60, s60, 4194304
  s_lshl_b32 s61, s51, 8
  s_add_u32 s61, s61, 6291456
  s_mov_b32 s63, 0x3f80
  s_mov_b32 s64, 0x3f800000
  v_lshlrev_b32 v6, 3, v13
  v_lshl_add_u32 v6, v14, 2, v6
  s_add_u32 m0, s40, 94208
  s_add_u32 s54, s55, 57344
  buffer_load_dwordx4 v242, s[16:19], s54 offen lds
  buffer_load_dwordx4 v243, s[16:19], s54 offen offset:1024 lds
  buffer_load_dwordx4 v244, s[16:19], s54 offen offset:2048 lds
  buffer_load_dwordx4 v245, s[16:19], s54 offen offset:3072 lds
  s_add_u32 m0, s40, 98304
  s_add_u32 s54, s55, 86016
  buffer_load_dwordx4 v242, s[16:19], s54 offen lds
  buffer_load_dwordx4 v243, s[16:19], s54 offen offset:1024 lds
  s_mov_b32 m0, 143360
  s_mov_b32 s54, 401408
  buffer_load_dwordx4 v242, s[16:19], s54 offen lds
  v_lshlrev_b32 v166, 7, v3
  v_cmp_gt_u32 vcc, 16, v3
  v_bfrev_b32 v165, 2
  s_nop 0
  v_cndmask_b32 v166, v165, v166, vcc
  s_waitcnt vmcnt(11)
  s_barrier
  buffer_store_dwordx2 v[18:19], v11, s[36:39], s82 offen sc1
  s_add_u32 s82, s82, 65536
  s_cmp_lg_u32 s76, 0
  s_cbranch_scc1 Lact
  s_waitcnt vmcnt(0)
  s_branch Lhh_done
Lact:
  s_add_u32 m0, s41, 0
  s_add_u32 s54, s58, 0
  buffer_load_dwordx4 v246, s[20:23], s54 offen lds
  buffer_load_dwordx4 v247, s[20:23], s54 offen offset:1024 lds
  buffer_load_dwordx4 v248, s[20:23], s54 offen offset:2048 lds
  buffer_load_dwordx4 v249, s[20:23], s54 offen offset:3072 lds
  s_add_u32 m0, s41, 4096
  s_add_u32 s54, s58, 57344
  buffer_load_dwordx4 v246, s[20:23], s54 offen lds
  buffer_load_dwordx4 v247, s[20:23], s54 offen offset:1024 lds
  buffer_load_dwordx4 v248, s[20:23], s54 offen offset:2048 lds
  s_add_u32 m0, s41, 28672
  s_add_u32 s54, s58, 401408
  buffer_load_dwordx4 v246, s[20:23], s54 offen lds
  buffer_load_dwordx4 v247, s[20:23], s54 offen offset:1024 lds
  s_waitcnt vmcnt(10)
  s_barrier
  ds_read_b64_tr_b16 a[64:65], v22 offset:0
  v_accvgpr_read_b32 v1, a251
  ds_read_b64_tr_b16 a[66:67], v22 offset:1024
  ds_read_b64_tr_b16 a[128:129], v23 offset:0
  ds_read_b64 v[194:195], v1
  ds_read_b64_tr_b16 a[130:131], v23 offset:1024
  ds_read_b64_tr_b16 a[68:69], v22 offset:2048
  buffer_load_dwordx4 v248, s[20:23], s54 offen offset:2048 lds
  v_xor_b32 v2, 16, v1
  ds_read_b64_tr_b16 a[70:71], v22 offset:3072
  ds_read_b64_tr_b16 a[132:133], v23 offset:2048
  ds_read_b64 v[196:197], v2
  ds_read_b64_tr_b16 a[134:135], v23 offset:3072
  ds_read_b64_tr_b16 a[72:73], v22 offset:3584
  buffer_load_dwordx4 v249, s[20:23], s54 offen offset:3072 lds
  v_xor_b32 v2, 32, v1
  ds_read_b64_tr_b16 a[74:75], v22 offset:4608
  ds_read_b64_tr_b16 a[136:137], v23 offset:3584
  ds_read_b64 v[198:199], v2
  ds_read_b64_tr_b16 a[138:139], v23 offset:4608
  ds_read_b64_tr_b16 a[76:77], v22 offset:5632
  s_add_u32 m0, s41, 32768
  s_add_u32 s54, s58, 458752
  buffer_load_dwordx4 v246, s[20:23], s54 offen lds
  v_xor_b32 v2, 48, v1
  ds_read_b64_tr_b16 a[78:79], v22 offset:6656
  ds_read_b64_tr_b16 a[140:141], v23 offset:5632
  ds_read_b64 v[200:201], v2
  ds_read_b64_tr_b16 a[142:143], v23 offset:6656
  ds_read_b64_tr_b16 a[80:81], v22 offset:7168
  buffer_load_dwordx4 v247, s[20:23], s54 offen offset:1024 lds
  s_waitcnt lgkmcnt(15)
  v_lshlrev_b32 v202, 16, v194
  ds_read_b64_tr_b16 a[82:83], v22 offset:8192
  ds_read_b64_tr_b16 a[144:145], v23 offset:7168
  v_and_b32 v203, 0xffff0000, v194
  ds_read_b64_tr_b16 a[146:147], v23 offset:8192
  ds_read_b64_tr_b16 a[84:85], v22 offset:9216
  buffer_load_dwordx4 v248, s[20:23], s54 offen offset:2048 lds
  v_lshlrev_b32 v204, 16, v195
  ds_read_b64_tr_b16 a[86:87], v22 offset:10240
  ds_read_b64_tr_b16 a[148:149], v23 offset:9216
  s_add_u32 m0, s41, 57344
  s_add_u32 s54, s58, 802816
  buffer_load_dwordx4 v246, s[20:23], s54 offen lds
  v_and_b32 v205, 0xffff0000, v195
  ds_read_b64_tr_b16 a[150:151], v23 offset:10240
  ds_read_b64_tr_b16 a[88:89], v22 offset:10752
  s_waitcnt lgkmcnt(15)
  v_lshlrev_b32 v206, 16, v196
  ds_read_b64_tr_b16 a[90:91], v22 offset:11776
  ds_read_b64_tr_b16 a[152:153], v23 offset:10752
  buffer_load_dwordx4 v247, s[20:23], s54 offen offset:1024 lds
  v_and_b32 v207, 0xffff0000, v196
  ds_read_b64_tr_b16 a[154:155], v23 offset:11776
  ds_read_b64_tr_b16 a[92:93], v22 offset:12800
  v_lshlrev_b32 v208, 16, v197
  ds_read_b64_tr_b16 a[94:95], v22 offset:13824
  ds_read_b64_tr_b16 a[156:157], v23 offset:12800
  buffer_load_dwordx4 v248, s[20:23], s54 offen offset:2048 lds
  v_and_b32 v209, 0xffff0000, v197
  ds_read_b64_tr_b16 a[158:159], v23 offset:13824
  ds_read_b64_tr_b16 a[96:97], v22 offset:14336
  buffer_load_dwordx4 v249, s[20:23], s54 offen offset:3072 lds
  v_lshlrev_b32 v210, 16, v198
  ds_read_b64_tr_b16 a[98:99], v22 offset:15360
  ds_read_b64_tr_b16 a[160:161], v23 offset:14336
  v_and_b32 v211, 0xffff0000, v198
  ds_read_b64_tr_b16 a[162:163], v23 offset:15360
  ds_read_b64_tr_b16 a[100:101], v22 offset:16384
  s_add_u32 m0, s41, 61440
  s_add_u32 s54, s58, 860160
  buffer_load_dwordx4 v246, s[20:23], s54 offen lds
  v_lshlrev_b32 v212, 16, v199
  ds_read_b64_tr_b16 a[102:103], v22 offset:17408
  ds_read_b64_tr_b16 a[164:165], v23 offset:16384
  buffer_load_dwordx4 v247, s[20:23], s54 offen offset:1024 lds
  buffer_load_dwordx2 v[162:163], v20, s[36:39], s83 offen sc1
  v_and_b32 v213, 0xffff0000, v199
  ds_read_b64_tr_b16 a[166:167], v23 offset:17408
  ds_read_b64_tr_b16 a[104:105], v22 offset:17920
  s_waitcnt lgkmcnt(15)
  v_lshlrev_b32 v214, 16, v200
  ds_read_b64_tr_b16 a[106:107], v22 offset:18944
  ds_read_b64_tr_b16 a[168:169], v23 offset:17920
  buffer_load_dwordx4 v248, s[20:23], s54 offen offset:2048 lds
  v_and_b32 v215, 0xffff0000, v200
  ds_read_b64_tr_b16 a[170:171], v23 offset:18944
  ds_read_b64_tr_b16 a[108:109], v22 offset:19968
  v_lshlrev_b32 v216, 16, v201
  ds_read_b64_tr_b16 a[110:111], v22 offset:20992
  ds_read_b64_tr_b16 a[172:173], v23 offset:19968
  v_and_b32 v217, 0xffff0000, v201
  ds_read_b64_tr_b16 a[174:175], v23 offset:20992
  ds_read_b64_tr_b16 a[112:113], v22 offset:21504
  ds_read_b64_tr_b16 a[114:115], v22 offset:22528
  ds_read_b64_tr_b16 a[176:177], v23 offset:21504
  ds_read_b64_tr_b16 a[178:179], v23 offset:22528
  ds_read_b64_tr_b16 a[116:117], v22 offset:23552
  ds_read_b64_tr_b16 a[118:119], v22 offset:24576
  ds_read_b64_tr_b16 a[180:181], v23 offset:23552
  ds_read_b64_tr_b16 a[182:183], v23 offset:24576
  ds_read_b64_tr_b16 a[120:121], v22 offset:25088
  ds_read_b64_tr_b16 a[122:123], v22 offset:26112
  ds_read_b64_tr_b16 a[184:185], v23 offset:25088
  ds_read_b64_tr_b16 a[186:187], v23 offset:26112
  ds_read_b64_tr_b16 a[124:125], v22 offset:27136
  ds_read_b64_tr_b16 a[126:127], v22 offset:28160
  ds_read_b64_tr_b16 a[188:189], v23 offset:27136
  ds_read_b64_tr_b16 a[190:191], v23 offset:28160
  s_waitcnt vmcnt(1)
  v_cmp_ne_u64 vcc, s[44:45], v[162:163]
  s_and_b64 vcc, s[14:15], vcc
  s_cbranch_vccz Lok1
  s_mov_b64 exec, s[14:15]
  s_mov_b32 s35, 1048576
Lpl1:
  buffer_load_dwordx2 v[162:163], v17, s[36:39], s83 offen sc1
  s_waitcnt vmcnt(0)
  v_cmp_ne_u64 vcc, s[44:45], v[162:163]
  s_and_b64 vcc, exec, vcc
  s_cbranch_vccz Lpx1
  s_sub_u32 s35, s35, 1
  s_cbranch_scc0 Lpl1
Lpx1:
  s_mov_b64 exec, -1
Lok1:
  s_add_u32 s83, s83, 65536
  s_waitcnt vmcnt(0)
  s_waitcnt lgkmcnt(0)
  s_barrier
  buffer_load_dwordx4 v[110:113], v12, s[36:39], s62 offen sc1
  v_add_u32 v24, 0, v21
  ds_read_b64_tr_b16 v[26:27], v24 offset:0
  ds_read_b64_tr_b16 v[28:29], v24 offset:2048
  ds_read_b64_tr_b16 v[30:31], v24 offset:4096
  ds_read_b64_tr_b16 v[32:33], v24 offset:6144
  ds_read_b64_tr_b16 v[34:35], v24 offset:8192
  ds_read_b64_tr_b16 v[36:37], v24 offset:10240
  ds_read_b64_tr_b16 v[38:39], v24 offset:12288
  ds_read_b64_tr_b16 v[40:41], v24 offset:14336
  ds_read_b64_tr_b16 v[42:43], v24 offset:16384
  ds_read_b64_tr_b16 v[44:45], v24 offset:18432
  buffer_load_dwordx4 v[114:117], v12, s[36:39], s62 offen offset:1024 sc1
  ds_read_b64_tr_b16 v[46:47], v24 offset:20480
  ds_read_b64_tr_b16 v[48:49], v24 offset:22528
  ds_read_b64_tr_b16 v[50:51], v24 offset:24576
  ds_read_b64_tr_b16 v[52:53], v24 offset:26624
  buffer_load_dwordx4 v[118:121], v12, s[36:39], s62 offen offset:2048 sc1
  v_add_u32 v24, 28672, v21
  ds_read_b64_tr_b16 v[54:55], v24 offset:0
  ds_read_b64_tr_b16 v[56:57], v24 offset:2048
  ds_read_b64_tr_b16 v[58:59], v24 offset:4096
  ds_read_b64_tr_b16 v[60:61], v24 offset:6144
  ds_read_b64_tr_b16 v[62:63], v24 offset:8192
  ds_read_b64_tr_b16 v[64:65], v24 offset:10240
  ds_read_b64_tr_b16 v[66:67], v24 offset:12288
  ds_read_b64_tr_b16 v[68:69], v24 offset:14336
  ds_read_b64_tr_b16 v[70:71], v24 offset:16384
  buffer_load_dwordx4 v[122:125], v12, s[36:39], s62 offen offset:3072 sc1
  ds_read_b64_tr_b16 v[72:73], v24 offset:18432
  ds_read_b64_tr_b16 v[74:75], v24 offset:20480
  ds_read_b64_tr_b16 v[76:77], v24 offset:22528
  ds_read_b64_tr_b16 v[78:79], v24 offset:24576
  ds_read_b64_tr_b16 v[80:81], v24 offset:26624
  s_add_u32 s62, s62, 4096
  buffer_load_dwordx4 v[126:129], v12, s[36:39], s62 offen sc1
  buffer_load_dwordx4 v[130:133], v12, s[36:39], s62 offen offset:1024 sc1
  buffer_load_dwordx4 v[134:137], v12, s[36:39], s62 offen offset:2048 sc1
  buffer_load_dwordx4 v[186:189], v12, s[36:39], s62 offen offset:3072 sc1
  s_add_u32 s62, s62, 4096
  buffer_load_dwordx4 a[192:195], v12, s[36:39], s62 offen sc1
  buffer_load_dwordx4 a[196:199], v12, s[36:39], s62 offen offset:1024 sc1
  s_or_b32 s51, %1, s49
  s_cmp_lg_u32 s51, 0
  s_cbranch_scc1 Lnoep
  v_mov_b32 v1, 0
  s_mov_b64 exec, 1
  buffer_store_dwordx2 v[18:19], v1, s[36:39], 0 offen sc1
  s_mov_b64 exec, -1
Lnoep:
  buffer_load_dwordx4 a[200:203], v12, s[36:39], s62 offen offset:2048 sc1
  s_waitcnt vmcnt(10)
  v_mfma_f32_32x32x16_bf16 v[146:161], a[128:131], v[110:113], 0
  v_mfma_f32_32x32x16_bf16 v[202:217], a[64:67], v[110:113], v[202:217]
  s_waitcnt vmcnt(9)
  v_mfma_f32_32x32x16_bf16 v[146:161], a[132:135], v[114:117], v[146:161]
  buffer_load_dwordx4 a[204:207], v12, s[36:39], s62 offen offset:3072 sc1
  v_mfma_f32_32x32x16_bf16 v[202:217], a[68:71], v[114:117], v[202:217]
  v_add_u32 v24, 57344, v21
  s_waitcnt vmcnt(9)
  v_mfma_f32_32x32x16_bf16 v[146:161], a[136:139], v[118:121], v[146:161]
  s_add_u32 s62, s62, 4096
  buffer_load_dwordx4 a[208:211], v12, s[36:39], s62 offen sc1
  v_mfma_f32_32x32x16_bf16 v[202:217], a[72:75], v[118:121], v[202:217]
  ds_read_b64_tr_b16 v[82:83], v24 offset:0
  buffer_load_dwordx4 a[212:215], v12, s[36:39], s62 offen offset:1024 sc1
  s_waitcnt vmcnt(10)
  v_mfma_f32_32x32x16_bf16 v[146:161], a[140:143], v[122:125], v[146:161]
  ds_read_b64_tr_b16 v[84:85], v24 offset:2048
  v_mfma_f32_32x32x16_bf16 v[202:217], a[76:79], v[122:125], v[202:217]
  buffer_load_dwordx4 a[216:219], v12, s[36:39], s62 offen offset:2048 sc1
  s_waitcnt vmcnt(10)
  v_mfma_f32_32x32x16_bf16 v[146:161], a[144:147], v[126:129], v[146:161]
  ds_read_b64_tr_b16 v[86:87], v24 offset:4096
  v_mfma_f32_32x32x16_bf16 v[202:217], a[80:83], v[126:129], v[202:217]
  ds_read_b64_tr_b16 v[88:89], v24 offset:6144
  buffer_load_dwordx4 a[220:223], v12, s[36:39], s62 offen offset:3072 sc1
  s_waitcnt vmcnt(10)
  v_mfma_f32_32x32x16_bf16 v[146:161], a[148:151], v[130:133], v[146:161]
  v_mfma_f32_32x32x16_bf16 v[202:217], a[84:87], v[130:133], v[202:217]
  ds_read_b64_tr_b16 v[90:91], v24 offset:8192
  s_add_u32 m0, s41, 86016
  s_add_u32 s54, s58, 1204224
  buffer_load_dwordx4 v246, s[20:23], s54 offen lds
  s_waitcnt vmcnt(10)
  v_mfma_f32_32x32x16_bf16 v[146:161], a[152:155], v[134:137], v[146:161]
  ds_read_b64_tr_b16 v[92:93], v24 offset:10240
  v_mfma_f32_32x32x16_bf16 v[202:217], a[88:91], v[134:137], v[202:217]
  buffer_load_dwordx4 v247, s[20:23], s54 offen offset:1024 lds
  s_waitcnt vmcnt(10)
  v_mfma_f32_32x32x16_bf16 v[146:161], a[156:159], v[186:189], v[146:161]
  ds_read_b64_tr_b16 v[94:95], v24 offset:12288
  v_mfma_f32_32x32x16_bf16 v[202:217], a[92:95], v[186:189], v[202:217]
  ds_read_b64_tr_b16 v[96:97], v24 offset:14336
  buffer_load_dwordx4 v248, s[20:23], s54 offen offset:2048 lds
  s_waitcnt vmcnt(10)
  v_mfma_f32_32x32x16_bf16 v[146:161], a[160:163], a[192:195], v[146:161]
  v_mfma_f32_32x32x16_bf16 v[202:217], a[96:99], a[192:195], v[202:217]
  ds_read_b64_tr_b16 v[98:99], v24 offset:16384
  buffer_load_dwordx4 v249, s[20:23], s54 offen offset:3072 lds
  s_waitcnt vmcnt(10)
  v_mfma_f32_32x32x16_bf16 v[146:161], a[164:167], a[196:199], v[146:161]
  ds_read_b64_tr_b16 v[100:101], v24 offset:18432
  v_mfma_f32_32x32x16_bf16 v[202:217], a[100:103], a[196:199], v[202:217]
  s_add_u32 m0, s41, 90112
  s_add_u32 s54, s58, 1261568
  buffer_load_dwordx4 v246, s[20:23], s54 offen lds
  s_waitcnt vmcnt(10)
  v_mfma_f32_32x32x16_bf16 v[146:161], a[168:171], a[200:203], v[146:161]
  ds_read_b64_tr_b16 v[102:103], v24 offset:20480
  v_mfma_f32_32x32x16_bf16 v[202:217], a[104:107], a[200:203], v[202:217]
  ds_read_b64_tr_b16 v[104:105], v24 offset:22528
  buffer_load_dwordx4 v247, s[20:23], s54 offen offset:1024 lds
  s_waitcnt vmcnt(10)
  v_mfma_f32_32x32x16_bf16 v[146:161], a[172:175], a[204:207], v[146:161]
  v_mfma_f32_32x32x16_bf16 v[202:217], a[108:111], a[204:207], v[202:217]
  ds_read_b64_tr_b16 v[106:107], v24 offset:24576
  buffer_load_dwordx4 v248, s[20:23], s54 offen offset:2048 lds
  s_waitcnt vmcnt(10)
  v_mfma_f32_32x32x16_bf16 v[146:161], a[176:179], a[208:211], v[146:161]
  ds_read_b64_tr_b16 v[108:109], v24 offset:26624
  v_mfma_f32_32x32x16_bf16 v[202:217], a[112:115], a[208:211], v[202:217]
  s_add_u32 m0, s42, 114688
  s_add_u32 s54, s59, 57344
  buffer_load_dwordx4 v242, s[24:27], s54 offen lds
  s_waitcnt vmcnt(10)
  v_mfma_f32_32x32x16_bf16 v[146:161], a[180:183], a[212:215], v[146:161]
  v_mfma_f32_32x32x16_bf16 v[202:217], a[116:119], a[212:215], v[202:217]
  buffer_load_dwordx4 v243, s[24:27], s54 offen offset:1024 lds
  s_waitcnt vmcnt(10)
  v_mfma_f32_32x32x16_bf16 v[146:161], a[184:187], a[216:219], v[146:161]
  v_mfma_f32_32x32x16_bf16 v[202:217], a[120:123], a[216:219], v[202:217]
  buffer_load_dwordx4 v244, s[24:27], s54 offen offset:2048 lds
  s_waitcnt vmcnt(10)
  v_mfma_f32_32x32x16_bf16 v[146:161], a[188:191], a[220:223], v[146:161]
  v_mfma_f32_32x32x16_bf16 v[202:217], a[124:127], a[220:223], v[202:217]
  buffer_load_dwordx4 v245, s[24:27], s54 offen offset:3072 lds
  v_mov_b32 v7, 0
  v_mov_b32 v9, 0
  v_mov_b32 v10, 0
  v_mov_b32 v2, 0
  s_nop 5
  v_cvt_pk_bf16_f32 v234, v146, v147
  v_cvt_pk_bf16_f32 v235, v148, v149
  v_cvt_pk_bf16_f32 v236, v150, v151
  v_cvt_pk_bf16_f32 v237, v152, v153
  ds_write_b128 v4, v[234:237]
  s_add_u32 m0, s42, 118784
  s_add_u32 s54, s59, 86016
  buffer_load_dwordx4 v242, s[24:27], s54 offen lds
  v_cvt_pk_bf16_f32 v238, v154, v155
  v_cvt_pk_bf16_f32 v239, v156, v157
  v_cvt_pk_bf16_f32 v240, v158, v159
  v_cvt_pk_bf16_f32 v241, v160, v161
  ds_write_b128 v4, v[238:241] offset:1024
  buffer_load_dwordx4 v243, s[24:27], s54 offen offset:1024 lds
  s_waitcnt lgkmcnt(0)
  s_barrier
  ds_read_b128 v[194:197], v5
  ds_read_b128 v[198:201], v5 offset:1024
  s_waitcnt lgkmcnt(1)
  v_dot2c_f32_bf16 v202, s63, v194
  v_dot2c_f32_bf16 v203, s64, v194
  v_dot2c_f32_bf16 v204, s63, v195
  v_dot2c_f32_bf16 v205, s64, v195
  v_dot2c_f32_bf16 v206, s63, v196
  v_dot2c_f32_bf16 v207, s64, v196
  v_dot2c_f32_bf16 v208, s63, v197
  v_dot2c_f32_bf16 v209, s64, v197
  v_cvt_pk_bf16_f32 v234, v202, v203
  v_cvt_pk_bf16_f32 v235, v204, v205
  v_cvt_pk_bf16_f32 v236, v206, v207
  v_cvt_pk_bf16_f32 v237, v208, v209
  buffer_store_dwordx4 v[234:237], v12, s[36:39], s60 offen sc1
  s_waitcnt lgkmcnt(0)
  v_dot2c_f32_bf16 v210, s63, v198
  v_dot2c_f32_bf16 v211, s64, v198
  v_dot2c_f32_bf16 v212, s63, v199
  v_dot2c_f32_bf16 v213, s64, v199
  v_dot2c_f32_bf16 v214, s63, v200
  v_dot2c_f32_bf16 v215, s64, v200
  v_dot2c_f32_bf16 v216, s63, v201
  v_dot2c_f32_bf16 v217, s64, v201
  v_cvt_pk_bf16_f32 v238, v210, v211
  v_cvt_pk_bf16_f32 v239, v212, v213
  v_cvt_pk_bf16_f32 v240, v214, v215
  v_cvt_pk_bf16_f32 v241, v216, v217
  buffer_store_dwordx4 v[238:241], v12, s[36:39], s60 offen offset:1024 sc1
  v_dot2c_f32_bf16 v7, s72, v234
  v_dot2c_f32_bf16 v9, s72, v235
  v_dot2c_f32_bf16 v7, s72, v236
  v_dot2c_f32_bf16 v9, s72, v237
  v_dot2c_f32_bf16 v10, v234, v234
  v_dot2c_f32_bf16 v2, v235, v235
  v_dot2c_f32_bf16 v10, v236, v236
  v_dot2c_f32_bf16 v2, v237, v237
  v_dot2c_f32_bf16 v7, s72, v238
  v_dot2c_f32_bf16 v9, s72, v239
  v_dot2c_f32_bf16 v7, s72, v240
  v_dot2c_f32_bf16 v9, s72, v241
  v_dot2c_f32_bf16 v10, v238, v238
  v_dot2c_f32_bf16 v2, v239, v239
  v_dot2c_f32_bf16 v10, v240, v240
  v_dot2c_f32_bf16 v2, v241, v241
  v_add_f32 v7, v7, v9
  s_nop 1
  v_add_f32 v10, v10, v2
  s_nop 1
  v_permlane32_swap_b32 v7, v10
  v_add_f32 v7, v7, v10
  buffer_store_dword v7, v6, s[36:39], s61 offen sc1
  buffer_load_dwordx4 v244, s[24:27], s54 offen offset:2048 lds
  v_mfma_f32_32x32x16_bf16 v[218:233], v[138:141], v[26:29], 0
  v_accvgpr_write_b32 a224, v202
  v_accvgpr_write_b32 a225, v203
  v_mfma_f32_32x32x16_bf16 v[218:233], v[138:141], v[30:33], v[218:233]
  v_accvgpr_write_b32 a226, v204
  v_accvgpr_write_b32 a227, v205
  v_accvgpr_write_b32 a228, v206
  v_mfma_f32_32x32x16_bf16 v[218:233], v[138:141], v[34:37], v[218:233]
  buffer_load_dwordx4 v245, s[24:27], s54 offen offset:3072 lds
  v_accvgpr_write_b32 a229, v207
  v_accvgpr_write_b32 a230, v208
  v_mfma_f32_32x32x16_bf16 v[218:233], v[138:141], v[38:41], v[218:233]
  s_add_u32 m0, s42, 0
  s_add_u32 s54, s59, 0
  buffer_load_dwordx4 v242, s[24:27], s54 offen lds
  v_accvgpr_write_b32 a231, v209
  v_accvgpr_write_b32 a232, v210
  v_accvgpr_write_b32 a233, v211
  v_mfma_f32_32x32x16_bf16 v[218:233], v[138:141], v[42:45], v[218:233]
  v_accvgpr_write_b32 a234, v212
  v_accvgpr_write_b32 a235, v213
  v_mfma_f32_32x32x16_bf16 v[218:233], v[138:141], v[46:49], v[218:233]
  buffer_load_dwordx4 v243, s[24:27], s54 offen offset:1024 lds
  v_accvgpr_write_b32 a236, v214
  v_accvgpr_write_b32 a237, v215
  v_accvgpr_write_b32 a238, v216
  v_mfma_f32_32x32x16_bf16 v[218:233], v[138:141], v[50:53], v[218:233]
  v_accvgpr_write_b32 a239, v217
  s_mul_i32 s62, s33, 114688
  v_mfma_f32_32x32x16_bf16 v[218:233], v[138:141], v[54:57], v[218:233]
  buffer_load_dwordx4 v244, s[24:27], s54 offen offset:2048 lds
  s_add_u32 s62, s62, s41
  s_add_u32 s62, s62, 4194304
  s_mul_i32 s66, s33, 448
  v_mfma_f32_32x32x16_bf16 v[218:233], v[138:141], v[58:61], v[218:233]
  s_lshl_b32 s51, s73, 3
  s_add_u32 s66, s66, s51
  v_mfma_f32_32x32x16_bf16 v[218:233], v[138:141], v[62:65], v[218:233]
  buffer_load_dwordx4 v245, s[24:27], s54 offen offset:3072 lds
  s_lshl_b32 s51, s49, 1
  s_add_u32 s66, s66, s51
  s_lshl_b32 s66, s66, 10
  v_mfma_f32_32x32x16_bf16 v[218:233], v[138:141], v[66:69], v[218:233]
  s_add_u32 s66, s66, 8388608
  s_lshl_b32 s51, s33, 2
  s_add_u32 m0, s42, 4096
  s_add_u32 s54, s59, 28672
  buffer_load_dwordx4 v242, s[24:27], s54 offen lds
  v_mfma_f32_32x32x16_bf16 v[218:233], v[138:141], v[70:73], v[218:233]
  s_add_u32 s51, s51, s49
  s_mul_i32 s67, s51, 3584
  s_add_u32 s67, s67, 6291456
  v_mfma_f32_32x32x16_bf16 v[218:233], v[138:141], v[74:77], v[218:233]
  v_add_u32 v169, 61440, v17
  v_lshlrev_b32 v170, 2, v13
  v_mfma_f32_32x32x16_bf16 v[218:233], v[138:141], v[78:81], v[218:233]
  buffer_load_dwordx4 v243, s[24:27], s54 offen offset:1024 lds
  v_add_u32 v170, 63488, v170
  v_mov_b32 v144, 0
  v_mov_b32 v145, 0
  v_mfma_f32_32x32x16_bf16 v[218:233], v[138:141], v[82:85], v[218:233]
  buffer_load_dwordx4 v244, s[24:27], s54 offen offset:2048 lds
  s_mov_b32 s69, 0
  s_mov_b32 s68, 0x1b
  v_mfma_f32_32x32x16_bf16 v[218:233], v[138:141], v[86:89], v[218:233]
  s_cmp_eq_u32 s49, 1
  s_cselect_b32 s68, 240, s68
  s_cmp_eq_u32 s49, 2
  v_mfma_f32_32x32x16_bf16 v[218:233], v[138:141], v[90:93], v[218:233]
  buffer_load_dwordx4 v245, s[24:27], s54 offen offset:3072 lds
  s_cselect_b32 s68, 3840, s68
  s_cmp_eq_u32 s49, 3
  v_mfma_f32_32x32x16_bf16 v[218:233], v[138:141], v[94:97], v[218:233]
  s_cselect_b32 s68, 55296, s68
  s_lshl_b32 s51, s33, 1
  s_add_u32 s51, s51, s74
  v_mfma_f32_32x32x16_bf16 v[218:233], v[138:141], v[98:101], v[218:233]
  s_mul_i32 s51, s51, 229376
  s_mul_i32 s71, s49, 28672
  v_mfma_f32_32x32x16_bf16 v[218:233], v[138:141], v[102:105], v[218:233]
  s_add_u32 s51, s51, s71
  s_add_u32 s70, s51, 8388608
  s_add_u32 s71, s70, 114688
  v_mfma_f32_32x32x16_bf16 v[218:233], v[138:141], v[106:109], v[218:233]
  s_lshl_b32 s51, s49, 5
  v_xor_b32 v22, s51, v8
  v_xor_b32 v23, 64, v22
  s_waitcnt vmcnt(10)
  s_barrier
  buffer_store_dwordx2 v[18:19], v11, s[36:39], s82 offen sc1
  s_add_u32 s82, s82, 65536
  v_add_u32 v24, 86016, v21
  ds_read_b64_tr_b16 v[110:111], v24 offset:0
  ds_read_b64_tr_b16 v[112:113], v24 offset:2048
  ds_read_b64_tr_b16 v[114:115], v24 offset:4096
  ds_read_b64_tr_b16 v[116:117], v24 offset:6144
  ds_read_b64_tr_b16 v[118:119], v24 offset:8192
  ds_read_b64_tr_b16 v[120:121], v24 offset:10240
  ds_read_b64_tr_b16 v[122:123], v24 offset:12288
  ds_read_b64_tr_b16 v[124:125], v24 offset:14336
  ds_read_b64_tr_b16 v[126:127], v24 offset:16384
  ds_read_b64_tr_b16 v[128:129], v24 offset:18432
  ds_read_b64_tr_b16 v[130:131], v24 offset:20480
  ds_read_b64_tr_b16 v[132:133], v24 offset:22528
  ds_read_b64_tr_b16 v[134:135], v24 offset:24576
  ds_read_b64_tr_b16 v[136:137], v24 offset:26624
  s_waitcnt lgkmcnt(12)
  v_mfma_f32_32x32x16_bf16 v[218:233], v[138:141], v[110:113], v[218:233]
  s_add_u32 s52, s42, 114688
  v_add_u32 v171, s52, v22
  v_add_u32 v176, s52, v23
  s_waitcnt lgkmcnt(10)
  v_mfma_f32_32x32x16_bf16 v[218:233], v[138:141], v[114:117], v[218:233]
  s_waitcnt vmcnt(9)
  ds_read_b64_tr_b16 a[32:33], v171 offset:0
  ds_read_b64_tr_b16 a[34:35], v171 offset:1024
  s_waitcnt lgkmcnt(10)
  v_mfma_f32_32x32x16_bf16 v[218:233], v[138:141], v[118:121], v[218:233]
  ds_read_b64_tr_b16 a[48:49], v176 offset:0
  ds_read_b64_tr_b16 a[50:51], v176 offset:1024
  ds_read_b64_tr_b16 a[36:37], v171 offset:2048
  s_waitcnt lgkmcnt(11)
  v_mfma_f32_32x32x16_bf16 v[218:233], v[138:141], v[122:125], v[218:233]
  ds_read_b64_tr_b16 a[38:39], v171 offset:3072
  ds_read_b64_tr_b16 a[52:53], v176 offset:2048
  ds_read_b64_tr_b16 a[54:55], v176 offset:3072
  s_waitcnt lgkmcnt(12)
  v_mfma_f32_32x32x16_bf16 v[218:233], v[138:141], v[126:129], v[218:233]
  ds_read_b64_tr_b16 a[40:41], v171 offset:4096
  ds_read_b64_tr_b16 a[42:43], v171 offset:5120
  ds_read_b64_tr_b16 a[56:57], v176 offset:4096
  s_waitcnt lgkmcnt(13)
  v_mfma_f32_32x32x16_bf16 v[218:233], v[138:141], v[130:133], v[218:233]
  ds_read_b64_tr_b16 a[58:59], v176 offset:5120
  ds_read_b64_tr_b16 a[44:45], v171 offset:6144
  ds_read_b64_tr_b16 a[46:47], v171 offset:7168
  s_waitcnt lgkmcnt(14)
  v_mfma_f32_32x32x16_bf16 v[218:233], v[138:141], v[134:137], v[218:233]
  ds_read_b64_tr_b16 a[60:61], v176 offset:6144
  ds_read_b64_tr_b16 a[62:63], v176 offset:7168
  buffer_load_dwordx2 v[162:163], v20, s[36:39], s83 offen sc1
  s_add_u32 s52, s42, 0
  v_add_u32 v171, s52, v22
  v_add_u32 v176, s52, v23
  s_waitcnt vmcnt(2)
  ds_read_b64_tr_b16 a[0:1], v171 offset:0
  ds_read_b64_tr_b16 a[2:3], v171 offset:1024
  ds_read_b64_tr_b16 a[16:17], v176 offset:0
  ds_read_b64_tr_b16 a[18:19], v176 offset:1024
  s_waitcnt lgkmcnt(4)
  ds_read_b64_tr_b16 a[4:5], v171 offset:2048
  ds_read_b64_tr_b16 a[6:7], v171 offset:3072
  ds_read_b64_tr_b16 a[20:21], v176 offset:2048
  ds_read_b64_tr_b16 a[22:23], v176 offset:3072
  ds_read_b64_tr_b16 a[8:9], v171 offset:4096
  ds_read_b64_tr_b16 a[10:11], v171 offset:5120
  ds_read_b64_tr_b16 a[24:25], v176 offset:4096
  s_add_u32 m0, s42, 114688
  s_add_u32 s54, s59, 114688
  buffer_load_dwordx4 v242, s[24:27], s54 offen lds
  ds_read_b64_tr_b16 a[26:27], v176 offset:5120
  ds_read_b64_tr_b16 a[12:13], v171 offset:6144
  ds_read_b64_tr_b16 a[14:15], v171 offset:7168
  ds_read_b64_tr_b16 a[28:29], v176 offset:6144
  ds_read_b64_tr_b16 a[30:31], v176 offset:7168
  buffer_load_dwordx4 v243, s[24:27], s54 offen offset:1024 lds
  v_cvt_pk_bf16_f32 v173, v218, 0
  v_lshlrev_b32 v1, 16, v173
  v_sub_f32 v1, v218, v1
  v_cvt_pk_bf16_f32 v172, v218, v1
  v_cndmask_b32 v172, v172, 0, s[46:47]
  v_cndmask_b32 v173, v173, 0, s[46:47]
  buffer_load_dwordx4 v244, s[24:27], s54 offen offset:2048 lds
  buffer_load_dwordx4 v245, s[24:27], s54 offen offset:3072 lds
  s_add_u32 m0, s42, 118784
  s_add_u32 s54, s59, 143360
  buffer_load_dwordx4 v242, s[24:27], s54 offen lds
  buffer_load_dwordx4 v243, s[24:27], s54 offen offset:1024 lds
  buffer_load_dwordx4 v244, s[24:27], s54 offen offset:2048 lds
  buffer_load_dwordx4 v245, s[24:27], s54 offen offset:3072 lds
  s_and_b32 s14, s14, 0xdffb
  s_waitcnt vmcnt(8)
  v_cmp_ne_u64 vcc, s[44:45], v[162:163]
  s_and_b64 vcc, s[14:15], vcc
  s_cbranch_vccz Lok2
  s_mov_b64 exec, s[14:15]
  s_mov_b32 s35, 1048576
Lpl2:
  buffer_load_dwordx2 v[162:163], v17, s[36:39], s83 offen sc1
  s_waitcnt vmcnt(0)
  v_cmp_ne_u64 vcc, s[44:45], v[162:163]
  s_and_b64 vcc, exec, vcc
  s_cbranch_vccz Lpx2
  s_sub_u32 s35, s35, 1
  s_cbranch_scc0 Lpl2
Lpx2:
  s_mov_b64 exec, -1
Lok2:
  s_add_u32 s83, s83, 65536
  s_waitcnt lgkmcnt(0)
  s_barrier
  s_add_u32 m0, s41, 4096
  s_add_u32 s54, s62, 0
  buffer_load_dwordx4 v12, s[36:39], s54 offen sc1 lds
  buffer_load_dwordx4 v12, s[36:39], s54 offen offset:1024 sc1 lds
  buffer_load_dwordx4 v12, s[36:39], s54 offen offset:2048 sc1 lds
  buffer_load_dwordx4 v12, s[36:39], s54 offen offset:3072 sc1 lds
  s_add_u32 m0, s41, 8192
  s_add_u32 s54, s62, 4096
  buffer_load_dwordx4 v12, s[36:39], s54 offen sc1 lds
  buffer_load_dwordx4 v12, s[36:39], s54 offen offset:1024 sc1 lds
  buffer_load_dwordx4 v12, s[36:39], s54 offen offset:2048 sc1 lds
  v_bfrev_b32 v1, 2
  s_and_b32 s14, s12, 0xdf00
  v_cndmask_b32 v20, v1, v11, s[14:15]
  s_and_b32 s14, s12, 0xfb
  v_cndmask_b32 v11, v1, v11, s[14:15]
  s_add_u32 m0, s56, 65536
  s_nop 0
  buffer_load_dwordx4 v12, s[36:39], s67 offen sc1 lds
  buffer_load_dwordx4 v12, s[36:39], s67 offen offset:1024 sc1 lds
  buffer_load_dwordx4 v12, s[36:39], s67 offen offset:2048 sc1 lds
  buffer_load_dwordx4 v12, s[36:39], s67 offen offset:3072 sc1 lds
  s_add_u32 m0, s41, 32768
  s_add_u32 s54, s62, 57344
  buffer_load_dwordx4 v12, s[36:39], s54 offen sc1 lds
  buffer_load_dwordx4 v12, s[36:39], s54 offen offset:1024 sc1 lds
  buffer_load_dwordx4 v12, s[36:39], s54 offen offset:2048 sc1 lds
  buffer_load_dwordx4 v12, s[36:39], s54 offen offset:3072 sc1 lds
  s_waitcnt vmcnt(8)
  s_barrier
  ds_read_b128 v[202:205], v12 offset:4096
  ds_read_b128 v[206:209], v12 offset:5120
  ds_read_b128 v[210:213], v12 offset:6144
  ds_read_b128 v[214:217], v12 offset:7168
  s_add_u32 m0, s41, 36864
  s_add_u32 s54, s62, 61440
  buffer_load_dwordx4 v12, s[36:39], s54 offen sc1 lds
  s_waitcnt lgkmcnt(3)
  v_mfma_f32_32x32x16_bf16 v[146:161], v[26:29], v[202:205], 0
  ds_read_b128 v[246:249], v12 offset:8192
  ds_read_b128 v[138:141], v12 offset:9216
  s_waitcnt lgkmcnt(4)
  v_mfma_f32_32x32x16_bf16 v[146:161], v[30:33], v[206:209], v[146:161]
  ds_read_b128 v[178:181], v12 offset:10240
  ds_read_b128 v[182:185], v12 offset:11264
  buffer_load_dwordx4 v12, s[36:39], s54 offen offset:1024 sc1 lds
  s_waitcnt lgkmcnt(4)
  v_mfma_f32_32x32x16_bf16 v[146:161], v[34:37], v[210:213], v[146:161]
  ds_read_b128 v[202:205], v12 offset:12288
  v_mfma_f32_32x32x16_bf16 v[146:161], v[38:41], v[214:217], v[146:161]
  ds_read_b128 v[206:209], v12 offset:13312
  buffer_load_dwordx4 v12, s[36:39], s54 offen offset:2048 sc1 lds
  s_waitcnt lgkmcnt(4)
  v_mfma_f32_32x32x16_bf16 v[146:161], v[42:45], v[246:249], v[146:161]
  ds_read_b128 v[210:213], v12 offset:14336
  v_mfma_f32_32x32x16_bf16 v[146:161], v[46:49], v[138:141], v[146:161]
  ds_read_b128 v[214:217], v12 offset:15360
  s_add_u32 m0, s42, 81920
  s_add_u32 s54, s59, 172032
  buffer_load_dwordx4 v242, s[24:27], s54 offen lds
  s_waitcnt lgkmcnt(4)
  v_mfma_f32_32x32x16_bf16 v[146:161], v[50:53], v[178:181], v[146:161]
  ds_read_b128 v[246:249], v12 offset:16384
  s_waitcnt vmcnt(8)
  v_lshlrev_b32 v1, 3, v13
  v_lshl_add_u32 v1, v14, 8, v1
  v_add_u32 v1, s56, v1
  v_mfma_f32_32x32x16_bf16 v[146:161], v[54:57], v[182:185], v[146:161]
  ds_read_b128 v[138:141], v12 offset:17408
  buffer_load_dwordx4 v243, s[24:27], s54 offen offset:1024 lds
  v_add_u32 v1, 65536, v1
  ds_read_b64 v[218:219], v1 offset:0
  ds_read_b64 v[220:221], v1 offset:512
  s_waitcnt lgkmcnt(6)
  v_mfma_f32_32x32x16_bf16 v[146:161], v[58:61], v[202:205], v[146:161]
  ds_read_b128 v[178:181], v12 offset:18432
  ds_read_b64 v[222:223], v1 offset:1024
  ds_read_b64 v[224:225], v1 offset:1536
  ds_read_b64 v[226:227], v1 offset:2048
  buffer_load_dwordx4 v244, s[24:27], s54 offen offset:2048 lds
  v_mfma_f32_32x32x16_bf16 v[146:161], v[62:65], v[206:209], v[146:161]
  ds_read_b128 v[182:185], v12 offset:19456
  ds_read_b64 v[228:229], v1 offset:2560
  ds_read_b64 v[230:231], v1 offset:3072
  s_waitcnt lgkmcnt(11)
  v_mfma_f32_32x32x16_bf16 v[146:161], v[66:69], v[210:213], v[146:161]
  ds_read_b128 v[202:205], v12 offset:20480
  buffer_load_dwordx4 v245, s[24:27], s54 offen offset:3072 lds
  s_waitcnt lgkmcnt(1)
  v_add_f32 v218, v218, v220
  v_add_f32 v219, v219, v221
  v_add_f32 v218, v218, v222
  v_add_f32 v219, v219, v223
  v_mfma_f32_32x32x16_bf16 v[146:161], v[70:73], v[214:217], v[146:161]
  ds_read_b128 v[206:209], v12 offset:21504
  v_add_f32 v218, v218, v224
  v_add_f32 v219, v219, v225
  v_add_f32 v218, v218, v226
  v_mfma_f32_32x32x16_bf16 v[146:161], v[74:77], v[246:249], v[146:161]
  ds_read_b128 v[210:213], v12 offset:22528
  s_add_u32 m0, s42, 86016
  s_add_u32 s54, s59, 200704
  buffer_load_dwordx4 v242, s[24:27], s54 offen lds
  v_add_f32 v219, v219, v227
  v_add_f32 v218, v218, v228
  v_add_f32 v219, v219, v229
  v_mfma_f32_32x32x16_bf16 v[146:161], v[78:81], v[138:141], v[146:161]
  ds_read_b128 v[214:217], v12 offset:23552
  v_add_f32 v218, v218, v230
  v_add_f32 v219, v219, v231
  s_nop 1
  v_permlane32_swap_b32 v218, v219
  v_add_f32 v218, v218, v219
  v_mfma_f32_32x32x16_bf16 v[146:161], v[82:85], v[178:181], v[146:161]
  ds_read_b128 v[246:249], v12 offset:24576
  buffer_load_dwordx4 v243, s[24:27], s54 offen offset:1024 lds
  v_mul_f32 v218, 0x3b124925, v218
  v_mov_b32 v219, v218
  s_nop 1
  v_permlane32_swap_b32 v218, v219
  v_mfma_f32_32x32x16_bf16 v[146:161], v[86:89], v[182:185], v[146:161]
  ds_read_b128 v[138:141], v12 offset:25600
  buffer_load_dwordx4 v244, s[24:27], s54 offen offset:2048 lds
  v_fma_f32 v221, -v218, v218, v219
  v_sub_f32 v220, 0, v218
  v_max_f32 v221, 0, v221
  s_waitcnt lgkmcnt(4)
  v_mfma_f32_32x32x16_bf16 v[146:161], v[90:93], v[202:205], v[146:161]
  ds_read_b128 v[178:181], v12 offset:26624
  v_add_f32 v221, 0x3727c5ac, v221
  v_rsq_f32 v221, v221
  v_cvt_pk_bf16_f32 v142, v220, v220
  v_mfma_f32_32x32x16_bf16 v[146:161], v[94:97], v[206:209], v[146:161]
  ds_read_b128 v[182:185], v12 offset:27648
  buffer_load_dwordx4 v245, s[24:27], s54 offen offset:3072 lds
  v_lshlrev_b32 v1, 16, v142
  v_sub_f32 v1, v220, v1
  v_cvt_pk_bf16_f32 v143, v1, 0
  s_waitcnt lgkmcnt(4)
  v_mfma_f32_32x32x16_bf16 v[146:161], v[98:101], v[210:213], v[146:161]
  ds_read_b128 v[202:205], v12 offset:28672
  v_cndmask_b32 v142, v142, 0, s[46:47]
  v_cndmask_b32 v143, v143, 0, s[46:47]
  v_mul_f32 v221, 0x3fb8aa3b, v221
  v_mfma_f32_32x32x16_bf16 v[146:161], v[102:105], v[214:217], v[146:161]
  ds_read_b128 v[206:209], v12 offset:29696
  s_lshl_b32 s51, s49, 9
  v_add_u32 v1, s51, v169
  ds_write_b64 v1, v[142:143]
  s_waitcnt lgkmcnt(5)
  v_mfma_f32_32x32x16_bf16 v[146:161], v[106:109], v[246:249], v[146:161]
  ds_read_b128 v[210:213], v12 offset:30720
  s_lshl_b32 s51, s49, 7
  v_add_u32 v1, s51, v170
  v_mfma_f32_32x32x16_bf16 v[146:161], v[110:113], v[138:141], v[146:161]
  ds_read_b128 v[214:217], v12 offset:31744
  ds_write_b32 v1, v221
  s_waitcnt lgkmcnt(6)
  v_mfma_f32_32x32x16_bf16 v[146:161], v[114:117], v[178:181], v[146:161]
  v_mfma_f32_32x32x16_bf16 v[146:161], v[118:121], v[182:185], v[146:161]
  s_waitcnt vmcnt(8)
  s_waitcnt lgkmcnt(0)
  s_barrier
  ds_read_b128 v[246:249], v12 offset:32768
  ds_read_b128 v[138:141], v12 offset:33792
  s_add_u32 m0, s41, 4096
  s_add_u32 s54, s62, 28672
  buffer_load_dwordx4 v12, s[36:39], s54 offen sc1 lds
  v_mfma_f32_32x32x16_bf16 v[146:161], v[122:125], v[202:205], v[146:161]
  ds_read_b128 v[178:181], v12 offset:34816
  v_mfma_f32_32x32x16_bf16 v[146:161], v[126:129], v[206:209], v[146:161]
  ds_read_b128 v[182:185], v12 offset:35840
  buffer_load_dwordx4 v12, s[36:39], s54 offen offset:1024 sc1 lds
  v_mfma_f32_32x32x16_bf16 v[146:161], v[130:133], v[210:213], v[146:161]
  ds_read_b128 v[202:205], v12 offset:36864
  ds_read_b64 v[142:143], v169 offset:0
  v_mfma_f32_32x32x16_bf16 v[146:161], v[134:137], v[214:217], v[146:161]
  ds_read_b128 v[206:209], v12 offset:37888
  buffer_load_dwordx4 v12, s[36:39], s54 offen offset:2048 sc1 lds
  buffer_load_dwordx4 v12, s[36:39], s54 offen offset:3072 sc1 lds
  s_waitcnt lgkmcnt(5)
  v_mfma_f32_32x32x16_bf16 v[186:201], v[26:29], v[246:249], 0
  s_waitcnt lgkmcnt(1)
  v_mfma_f32_32x32x16_bf16 v[146:161], v[172:175], v[142:145], v[146:161]
  ds_read_b128 v[210:213], v12 offset:38912
  s_add_u32 m0, s41, 8192
  s_add_u32 s54, s62, 32768
  buffer_load_dwordx4 v12, s[36:39], s54 offen sc1 lds
  v_mfma_f32_32x32x16_bf16 v[186:201], v[30:33], v[138:141], v[186:201]
  ds_read_b128 v[214:217], v12 offset:39936
  ds_read_b32 v168, v170 offset:0
  v_mfma_f32_32x32x16_bf16 v[186:201], v[34:37], v[178:181], v[186:201]
  ds_read_b128 v[246:249], v12 offset:40960
  buffer_load_dwordx4 v12, s[36:39], s54 offen offset:1024 sc1 lds
  s_waitcnt lgkmcnt(1)
  s_nop 0
  v_mul_f32 v146, v146, v168
  v_exp_f32 v218, -v146
  v_mul_f32 v147, v147, v168
  v_exp_f32 v219, -v147
  v_add_f32 v218, 1.0, v218
  v_mfma_f32_32x32x16_bf16 v[186:201], v[38:41], v[182:185], v[186:201]
  ds_read_b128 v[138:141], v12 offset:41984
  v_rcp_f32 v218, v218
  v_mul_f32 v148, v148, v168
  v_exp_f32 v220, -v148
  v_add_f32 v219, 1.0, v219
  v_mfma_f32_32x32x16_bf16 v[186:201], v[42:45], v[202:205], v[186:201]
  ds_read_b128 v[178:181], v12 offset:43008
  buffer_load_dwordx4 v12, s[36:39], s54 offen offset:2048 sc1 lds
  v_rcp_f32 v219, v219
  v_mul_f32 v146, v146, v218
  v_mul_f32 v149, v149, v168
  v_exp_f32 v221, -v149
  v_mfma_f32_32x32x16_bf16 v[186:201], v[46:49], v[206:209], v[186:201]
  ds_read_b128 v[182:185], v12 offset:44032
  s_add_u32 m0, s43, 0
  s_add_u32 s54, s59, 229376
  buffer_load_dwordx4 v242, s[24:27], s54 offen lds
  v_add_f32 v220, 1.0, v220
  v_rcp_f32 v220, v220
  v_mul_f32 v147, v147, v219
  v_cvt_pk_bf16_f32 v234, v146, v147
  v_mul_f32 v150, v150, v168
  v_exp_f32 v222, -v150
  v_mfma_f32_32x32x16_bf16 v[186:201], v[50:53], v[210:213], v[186:201]
  ds_read_b128 v[202:205], v12 offset:45056
  buffer_load_dwordx4 v243, s[24:27], s54 offen offset:1024 lds
  v_add_f32 v221, 1.0, v221
  v_rcp_f32 v221, v221
  v_mul_f32 v148, v148, v220
  v_mul_f32 v151, v151, v168
  v_mfma_f32_32x32x16_bf16 v[186:201], v[54:57], v[214:217], v[186:201]
  ds_read_b128 v[206:209], v12 offset:46080
  buffer_load_dwordx4 v244, s[24:27], s54 offen offset:2048 lds
  v_exp_f32 v223, -v151
  v_add_f32 v222, 1.0, v222
  v_rcp_f32 v222, v222
  v_mul_f32 v149, v149, v221
  s_waitcnt lgkmcnt(4)
  v_mfma_f32_32x32x16_bf16 v[186:201], v[58:61], v[246:249], v[186:201]
  ds_read_b128 v[210:213], v12 offset:47104
  v_cvt_pk_bf16_f32 v235, v148, v149
  v_mul_f32 v152, v152, v168
  v_exp_f32 v224, -v152
  v_add_f32 v223, 1.0, v223
  v_rcp_f32 v223, v223
  v_mfma_f32_32x32x16_bf16 v[186:201], v[62:65], v[138:141], v[186:201]
  ds_read_b128 v[214:217], v12 offset:48128
  buffer_load_dwordx4 v245, s[24:27], s54 offen offset:3072 lds
  v_mul_f32 v150, v150, v222
  v_mul_f32 v153, v153, v168
  v_exp_f32 v225, -v153
  v_add_f32 v224, 1.0, v224
  v_rcp_f32 v224, v224
  s_waitcnt lgkmcnt(4)
  v_mfma_f32_32x32x16_bf16 v[186:201], v[66:69], v[178:181], v[186:201]
  ds_read_b128 v[246:249], v12 offset:49152
  s_add_u32 m0, s43, 4096
  s_add_u32 s54, s59, 258048
  buffer_load_dwordx4 v242, s[24:27], s54 offen lds
  v_mul_f32 v151, v151, v223
  v_cvt_pk_bf16_f32 v236, v150, v151
  v_mul_f32 v154, v154, v168
  v_exp_f32 v226, -v154
  v_mfma_f32_32x32x16_bf16 v[186:201], v[70:73], v[182:185], v[186:201]
  ds_read_b128 v[138:141], v12 offset:50176
  buffer_load_dwordx4 v243, s[24:27], s54 offen offset:1024 lds
  v_add_f32 v225, 1.0, v225
  v_rcp_f32 v225, v225
  v_mul_f32 v152, v152, v224
  v_mul_f32 v155, v155, v168
  v_exp_f32 v227, -v155
  s_waitcnt lgkmcnt(4)
  v_mfma_f32_32x32x16_bf16 v[186:201], v[74:77], v[202:205], v[186:201]
  ds_read_b128 v[178:181], v12 offset:51200
  buffer_load_dwordx4 v244, s[24:27], s54 offen offset:2048 lds
  v_add_f32 v226, 1.0, v226
  v_rcp_f32 v226, v226
  v_mul_f32 v153, v153, v225
  v_cvt_pk_bf16_f32 v237, v152, v153
  v_mfma_f32_32x32x16_bf16 v[186:201], v[78:81], v[206:209], v[186:201]
  ds_read_b128 v[182:185], v12 offset:52224
  s_add_u32 s51, s66, 0
  s_waitcnt lgkmcnt(4)
  v_mfma_f32_32x32x16_bf16 v[186:201], v[82:85], v[210:213], v[186:201]
  ds_read_b128 v[202:205], v12 offset:53248
  buffer_load_dwordx4 v245, s[24:27], s54 offen offset:3072 lds
  buffer_store_dwordx4 v[234:237], v12, s[36:39], s51 offen sc1
  v_mul_f32 v156, v156, v168
  v_exp_f32 v228, -v156
  v_add_f32 v227, 1.0, v227
  v_rcp_f32 v227, v227
  v_mfma_f32_32x32x16_bf16 v[186:201], v[86:89], v[214:217], v[186:201]
  ds_read_b128 v[206:209], v12 offset:54272
  v_mul_f32 v154, v154, v226
  v_mul_f32 v157, v157, v168
  v_exp_f32 v229, -v157
  v_add_f32 v228, 1.0, v228
  v_rcp_f32 v228, v228
  s_waitcnt lgkmcnt(4)
  v_mfma_f32_32x32x16_bf16 v[186:201], v[90:93], v[246:249], v[186:201]
  ds_read_b128 v[210:213], v12 offset:55296
  v_mul_f32 v155, v155, v227
  v_cvt_pk_bf16_f32 v238, v154, v155
  v_mul_f32 v158, v158, v168
  v_exp_f32 v230, -v158
  v_mfma_f32_32x32x16_bf16 v[186:201], v[94:97], v[138:141], v[186:201]
  ds_read_b128 v[214:217], v12 offset:56320
  v_add_f32 v229, 1.0, v229
  v_rcp_f32 v229, v229
  v_mul_f32 v156, v156, v228
  v_mul_f32 v159, v159, v168
  v_exp_f32 v231, -v159
  s_waitcnt lgkmcnt(4)
  v_mfma_f32_32x32x16_bf16 v[186:201], v[98:101], v[178:181], v[186:201]
  ds_read_b128 v[246:249], v12 offset:57344
  v_add_f32 v230, 1.0, v230
  v_rcp_f32 v230, v230
  v_mul_f32 v157, v157, v229
  v_cvt_pk_bf16_f32 v239, v156, v157
  v_mul_f32 v160, v160, v168
  v_mfma_f32_32x32x16_bf16 v[186:201], v[102:105], v[182:185], v[186:201]
  ds_read_b128 v[138:141], v12 offset:58368
  v_exp_f32 v232, -v160
  v_add_f32 v231, 1.0, v231
  v_rcp_f32 v231, v231
  v_mul_f32 v158, v158, v230
  s_waitcnt lgkmcnt(4)
  v_mfma_f32_32x32x16_bf16 v[186:201], v[106:109], v[202:205], v[186:201]
  ds_read_b128 v[178:181], v12 offset:59392
  v_mul_f32 v161, v161, v168
  v_exp_f32 v233, -v161
  v_add_f32 v232, 1.0, v232
  v_rcp_f32 v232, v232
  v_mfma_f32_32x32x16_bf16 v[186:201], v[110:113], v[206:209], v[186:201]
  ds_read_b128 v[182:185], v12 offset:60416
  v_mul_f32 v159, v159, v231
  v_cvt_pk_bf16_f32 v240, v158, v159
  v_add_f32 v233, 1.0, v233
  v_rcp_f32 v233, v233
  v_mul_f32 v160, v160, v232
  v_mul_f32 v161, v161, v233
  s_waitcnt lgkmcnt(4)
  v_mfma_f32_32x32x16_bf16 v[186:201], v[114:117], v[210:213], v[186:201]
  v_cvt_pk_bf16_f32 v241, v160, v161
  buffer_store_dwordx4 v[238:241], v12, s[36:39], s51 offen offset:1024 sc1
  v_mfma_f32_32x32x16_bf16 v[186:201], v[118:121], v[214:217], v[186:201]
  s_waitcnt vmcnt(10)
  s_waitcnt lgkmcnt(0)
  s_barrier
  ds_read_b128 v[202:205], v12 offset:4096
  ds_read_b128 v[206:209], v12 offset:5120
  s_add_u32 m0, s41, 32768
  s_add_u32 s54, s62, 86016
  buffer_load_dwordx4 v12, s[36:39], s54 offen sc1 lds
  s_add_u32 s52, s42, 114688
  v_add_u32 v171, s52, v22
  v_add_u32 v176, s52, v23
  ds_read_b64_tr_b16 a[64:65], v171 offset:0
  buffer_load_dwordx4 v12, s[36:39], s54 offen offset:1024 sc1 lds
  v_mfma_f32_32x32x16_bf16 v[186:201], v[122:125], v[246:249], v[186:201]
  ds_read_b128 v[210:213], v12 offset:6144
  ds_read_b64_tr_b16 a[66:67], v171 offset:1024
  ds_read_b64_tr_b16 a[80:81], v176 offset:0
  ds_read_b64_tr_b16 a[82:83], v176 offset:1024
  ds_read_b64_tr_b16 a[68:69], v171 offset:2048
  ds_read_b64_tr_b16 a[70:71], v171 offset:3072
  v_mfma_f32_32x32x16_bf16 v[186:201], v[126:129], v[138:141], v[186:201]
  ds_read_b128 v[214:217], v12 offset:7168
  ds_read_b64_tr_b16 a[84:85], v176 offset:2048
  ds_read_b64_tr_b16 a[86:87], v176 offset:3072
  ds_read_b64_tr_b16 a[72:73], v171 offset:4096
  ds_read_b64_tr_b16 a[74:75], v171 offset:5120
  ds_read_b64_tr_b16 a[88:89], v176 offset:4096
  buffer_load_dwordx4 v12, s[36:39], s54 offen offset:2048 sc1 lds
  v_mfma_f32_32x32x16_bf16 v[186:201], v[130:133], v[178:181], v[186:201]
  ds_read_b128 v[246:249], v12 offset:8192
  ds_read_b64 v[142:143], v169 offset:1024
  ds_read_b64_tr_b16 a[90:91], v176 offset:5120
  ds_read_b64_tr_b16 a[76:77], v171 offset:6144
  ds_read_b64_tr_b16 a[78:79], v171 offset:7168
  ds_read_b64_tr_b16 a[92:93], v176 offset:6144
  ds_read_b64_tr_b16 a[94:95], v176 offset:7168
  v_mfma_f32_32x32x16_bf16 v[186:201], v[134:137], v[182:185], v[186:201]
  ds_read_b128 v[138:141], v12 offset:9216
  buffer_load_dwordx4 v12, s[36:39], s54 offen offset:3072 sc1 lds
  s_waitcnt lgkmcnt(15)
  v_mfma_f32_32x32x16_bf16 v[146:161], v[26:29], v[202:205], 0
  s_waitcnt lgkmcnt(6)
  v_mfma_f32_32x32x16_bf16 v[186:201], v[172:175], v[142:145], v[186:201]
  ds_read_b128 v[178:181], v12 offset:10240
  s_add_u32 m0, s41, 36864
  s_add_u32 s54, s62, 90112
  buffer_load_dwordx4 v12, s[36:39], s54 offen sc1 lds
  v_mfma_f32_32x32x16_bf16 v[146:161], v[30:33], v[206:209], v[146:161]
  ds_read_b128 v[182:185], v12 offset:11264
  ds_read_b32 v168, v170 offset:256
  v_mfma_f32_32x32x16_bf16 v[146:161], v[34:37], v[210:213], v[146:161]
  ds_read_b128 v[202:205], v12 offset:12288
  buffer_load_dwordx4 v12, s[36:39], s54 offen offset:1024 sc1 lds
  s_waitcnt lgkmcnt(1)
  s_nop 0
  v_mul_f32 v186, v186, v168
  v_exp_f32 v218, -v186
  v_mul_f32 v187, v187, v168
  v_exp_f32 v219, -v187
  v_mfma_f32_32x32x16_bf16 v[146:161], v[38:41], v[214:217], v[146:161]
  ds_read_b128 v[206:209], v12 offset:13312
  v_add_f32 v218, 1.0, v218
  v_rcp_f32 v218, v218
  v_mul_f32 v188, v188, v168
  v_exp_f32 v220, -v188
  v_mfma_f32_32x32x16_bf16 v[146:161], v[42:45], v[246:249], v[146:161]
  ds_read_b128 v[210:213], v12 offset:14336
  buffer_load_dwordx4 v12, s[36:39], s54 offen offset:2048 sc1 lds
  v_add_f32 v219, 1.0, v219
  v_rcp_f32 v219, v219
  v_mul_f32 v186, v186, v218
  v_mul_f32 v189, v189, v168
  v_exp_f32 v221, -v189
  v_mfma_f32_32x32x16_bf16 v[146:161], v[46:49], v[138:141], v[146:161]
  ds_read_b128 v[214:217], v12 offset:15360
  s_add_u32 m0, s42, 114688
  s_add_u32 s54, s59, 286720
  buffer_load_dwordx4 v242, s[24:27], s54 offen lds
  v_add_f32 v220, 1.0, v220
  v_rcp_f32 v220, v220
  v_mul_f32 v187, v187, v219
  v_mfma_f32_32x32x16_bf16 v[146:161], v[50:53], v[178:181], v[146:161]
  ds_read_b128 v[246:249], v12 offset:16384
  buffer_load_dwordx4 v243, s[24:27], s54 offen offset:1024 lds
  v_cvt_pk_bf16_f32 v234, v186, v187
  v_mul_f32 v190, v190, v168
  v_exp_f32 v222, -v190
  v_add_f32 v221, 1.0, v221
  v_rcp_f32 v221, v221
  v_mfma_f32_32x32x16_bf16 v[146:161], v[54:57], v[182:185], v[146:161]
  ds_read_b128 v[138:141], v12 offset:17408
  buffer_load_dwordx4 v244, s[24:27], s54 offen offset:2048 lds
  v_mul_f32 v188, v188, v220
  v_mul_f32 v191, v191, v168
  v_exp_f32 v223, -v191
  v_add_f32 v222, 1.0, v222
  s_waitcnt lgkmcnt(4)
  v_mfma_f32_32x32x16_bf16 v[146:161], v[58:61], v[202:205], v[146:161]
  ds_read_b128 v[178:181], v12 offset:18432
  v_rcp_f32 v222, v222
  v_mul_f32 v189, v189, v221
  v_cvt_pk_bf16_f32 v235, v188, v189
  v_mul_f32 v192, v192, v168
  v_exp_f32 v224, -v192
  v_mfma_f32_32x32x16_bf16 v[146:161], v[62:65], v[206:209], v[146:161]
  ds_read_b128 v[182:185], v12 offset:19456
  buffer_load_dwordx4 v245, s[24:27], s54 offen offset:3072 lds
  v_add_f32 v223, 1.0, v223
  v_rcp_f32 v223, v223
  v_mul_f32 v190, v190, v222
  s_waitcnt lgkmcnt(4)
  v_mfma_f32_32x32x16_bf16 v[146:161], v[66:69], v[210:213], v[146:161]
  ds_read_b128 v[202:205], v12 offset:20480
  s_add_u32 m0, s42, 118784
  s_add_u32 s54, s59, 315392
  buffer_load_dwordx4 v242, s[24:27], s54 offen lds
  v_mul_f32 v193, v193, v168
  v_exp_f32 v225, -v193
  v_add_f32 v224, 1.0, v224
  v_rcp_f32 v224, v224
  v_mfma_f32_32x32x16_bf16 v[146:161], v[70:73], v[214:217], v[146:161]
  ds_read_b128 v[206:209], v12 offset:21504
  buffer_load_dwordx4 v243, s[24:27], s54 offen offset:1024 lds
  v_mul_f32 v191, v191, v223
  v_cvt_pk_bf16_f32 v236, v190, v191
  v_mul_f32 v194, v194, v168
  v_exp_f32 v226, -v194
  v_add_f32 v225, 1.0, v225
  s_waitcnt lgkmcnt(4)
  v_mfma_f32_32x32x16_bf16 v[146:161], v[74:77], v[246:249], v[146:161]
  ds_read_b128 v[210:213], v12 offset:22528
  v_rcp_f32 v225, v225
  v_mul_f32 v192, v192, v224
  v_mul_f32 v195, v195, v168
  v_exp_f32 v227, -v195
  v_mfma_f32_32x32x16_bf16 v[146:161], v[78:81], v[138:141], v[146:161]
  ds_read_b128 v[214:217], v12 offset:23552
  buffer_load_dwordx4 v244, s[24:27], s54 offen offset:2048 lds
  v_add_f32 v226, 1.0, v226
  v_rcp_f32 v226, v226
  v_mul_f32 v193, v193, v225
  v_cvt_pk_bf16_f32 v237, v192, v193
  s_waitcnt lgkmcnt(4)
  v_mfma_f32_32x32x16_bf16 v[146:161], v[82:85], v[178:181], v[146:161]
  ds_read_b128 v[246:249], v12 offset:24576
  buffer_load_dwordx4 v245, s[24:27], s54 offen offset:3072 lds
  s_add_u32 s51, s66, 229376
  v_mfma_f32_32x32x16_bf16 v[146:161], v[86:89], v[182:185], v[146:161]
  ds_read_b128 v[138:141], v12 offset:25600
  buffer_store_dwordx4 v[234:237], v12, s[36:39], s51 offen sc1
  v_mul_f32 v196, v196, v168
  v_exp_f32 v228, -v196
  v_add_f32 v227, 1.0, v227
  v_rcp_f32 v227, v227
  s_waitcnt lgkmcnt(4)
  v_mfma_f32_32x32x16_bf16 v[146:161], v[90:93], v[202:205], v[146:161]
  ds_read_b128 v[178:181], v12 offset:26624
  v_mul_f32 v194, v194, v226
  v_mul_f32 v197, v197, v168
  v_exp_f32 v229, -v197
  v_mfma_f32_32x32x16_bf16 v[146:161], v[94:97], v[206:209], v[146:161]
  ds_read_b128 v[182:185], v12 offset:27648
  v_add_f32 v228, 1.0, v228
  v_rcp_f32 v228, v228
  v_mul_f32 v195, v195, v227
  v_cvt_pk_bf16_f32 v238, v194, v195
  v_mul_f32 v198, v198, v168
  s_waitcnt lgkmcnt(4)
  v_mfma_f32_32x32x16_bf16 v[146:161], v[98:101], v[210:213], v[146:161]
  ds_read_b128 v[202:205], v12 offset:28672
  v_exp_f32 v230, -v198
  v_add_f32 v229, 1.0, v229
  s_waitcnt vmcnt(16)
  s_barrier
  v_mfma_f32_32x32x16_bf16 v[146:161], v[102:105], v[214:217], v[146:161]
  ds_read_b128 v[206:209], v12 offset:29696
  buffer_store_dwordx2 v[18:19], v11, s[36:39], s82 offen sc1
  s_waitcnt lgkmcnt(4)
  v_mfma_f32_32x32x16_bf16 v[146:161], v[106:109], v[246:249], v[146:161]
  ds_read_b128 v[210:213], v12 offset:30720
  v_rcp_f32 v229, v229
  v_mul_f32 v196, v196, v228
  v_mul_f32 v199, v199, v168
  v_mfma_f32_32x32x16_bf16 v[146:161], v[110:113], v[138:141], v[146:161]
  ds_read_b128 v[214:217], v12 offset:31744
  v_exp_f32 v231, -v199
  v_add_f32 v230, 1.0, v230
  v_rcp_f32 v230, v230
  v_mul_f32 v197, v197, v229
  s_waitcnt lgkmcnt(4)
  v_mfma_f32_32x32x16_bf16 v[146:161], v[114:117], v[178:181], v[146:161]
  v_cvt_pk_bf16_f32 v239, v196, v197
  v_mul_f32 v200, v200, v168
  v_exp_f32 v232, -v200
  v_add_f32 v231, 1.0, v231
  v_rcp_f32 v231, v231
  v_mfma_f32_32x32x16_bf16 v[146:161], v[118:121], v[182:185], v[146:161]
  s_waitcnt vmcnt(10)
  s_waitcnt lgkmcnt(0)
  s_barrier
  ds_read_b128 v[246:249], v12 offset:32768
  ds_read_b128 v[138:141], v12 offset:33792
  v_mul_f32 v198, v198, v230
  v_mul_f32 v201, v201, v168
  v_exp_f32 v233, -v201
  v_mfma_f32_32x32x16_bf16 v[146:161], v[122:125], v[202:205], v[146:161]
  ds_read_b128 v[178:181], v12 offset:34816
  v_add_f32 v232, 1.0, v232
  v_rcp_f32 v232, v232
  v_mul_f32 v199, v199, v231
  v_cvt_pk_bf16_f32 v240, v198, v199
  v_add_f32 v233, 1.0, v233
  v_mfma_f32_32x32x16_bf16 v[146:161], v[126:129], v[206:209], v[146:161]
  ds_read_b128 v[182:185], v12 offset:35840
  v_rcp_f32 v233, v233
  v_mul_f32 v200, v200, v232
  v_mul_f32 v201, v201, v233
  v_cvt_pk_bf16_f32 v241, v200, v201
  v_mfma_f32_32x32x16_bf16 v[146:161], v[130:133], v[210:213], v[146:161]
  ds_read_b128 v[202:205], v12 offset:36864
  ds_read_b64 v[142:143], v169 offset:512
  buffer_store_dwordx4 v[238:241], v12, s[36:39], s51 offen offset:1024 sc1
  v_mfma_f32_32x32x16_bf16 v[146:161], v[134:137], v[214:217], v[146:161]
  ds_read_b128 v[206:209], v12 offset:37888
  s_add_u32 m0, s42, 0
  s_add_u32 s54, s59, 344064
  buffer_load_dwordx4 v242, s[24:27], s54 offen lds
  s_waitcnt lgkmcnt(5)
  v_mfma_f32_32x32x16_bf16 v[186:201], v[26:29], v[246:249], 0
  s_waitcnt lgkmcnt(1)
  v_mfma_f32_32x32x16_bf16 v[146:161], v[172:175], v[142:145], v[146:161]
  ds_read_b128 v[210:213], v12 offset:38912
  buffer_load_dwordx4 v243, s[24:27], s54 offen offset:1024 lds
  v_mfma_f32_32x32x16_bf16 v[186:201], v[30:33], v[138:141], v[186:201]
  ds_read_b128 v[214:217], v12 offset:39936
  ds_read_b32 v168, v170 offset:128
  v_mfma_f32_32x32x16_bf16 v[186:201], v[34:37], v[178:181], v[186:201]
  ds_read_b128 v[246:249], v12 offset:40960
  buffer_load_dwordx4 v244, s[24:27], s54 offen offset:2048 lds
  s_waitcnt lgkmcnt(1)
  s_nop 2
  v_mul_f32 v146, v146, v168
  v_exp_f32 v218, -v146
  v_mul_f32 v147, v147, v168
  v_exp_f32 v219, -v147
  v_mfma_f32_32x32x16_bf16 v[186:201], v[38:41], v[182:185], v[186:201]
  ds_read_b128 v[138:141], v12 offset:41984
  v_add_f32 v218, 1.0, v218
  v_rcp_f32 v218, v218
  v_mul_f32 v148, v148, v168
  v_exp_f32 v220, -v148
  v_mfma_f32_32x32x16_bf16 v[186:201], v[42:45], v[202:205], v[186:201]
  ds_read_b128 v[178:181], v12 offset:43008
  buffer_load_dwordx4 v245, s[24:27], s54 offen offset:3072 lds
  v_add_f32 v219, 1.0, v219
  v_rcp_f32 v219, v219
  v_mul_f32 v146, v146, v218
  v_mul_f32 v149, v149, v168
  v_exp_f32 v221, -v149
  v_mfma_f32_32x32x16_bf16 v[186:201], v[46:49], v[206:209], v[186:201]
  ds_read_b128 v[182:185], v12 offset:44032
  s_add_u32 m0, s42, 4096
  s_add_u32 s54, s59, 372736
  buffer_load_dwordx4 v242, s[24:27], s54 offen lds
  v_add_f32 v220, 1.0, v220
  v_rcp_f32 v220, v220
  v_mul_f32 v147, v147, v219
  v_mfma_f32_32x32x16_bf16 v[186:201], v[50:53], v[210:213], v[186:201]
  ds_read_b128 v[202:205], v12 offset:45056
  buffer_load_dwordx4 v243, s[24:27], s54 offen offset:1024 lds
  v_cvt_pk_bf16_f32 v234, v146, v147
  v_mul_f32 v150, v150, v168
  v_exp_f32 v222, -v150
  v_add_f32 v221, 1.0, v221
  v_rcp_f32 v221, v221
  v_mfma_f32_32x32x16_bf16 v[186:201], v[54:57], v[214:217], v[186:201]
  ds_read_b128 v[206:209], v12 offset:46080
  buffer_load_dwordx4 v244, s[24:27], s54 offen offset:2048 lds
  v_mul_f32 v148, v148, v220
  v_mul_f32 v151, v151, v168
  v_exp_f32 v223, -v151
  v_add_f32 v222, 1.0, v222
  s_waitcnt lgkmcnt(4)
  v_mfma_f32_32x32x16_bf16 v[186:201], v[58:61], v[246:249], v[186:201]
  ds_read_b128 v[210:213], v12 offset:47104
  v_rcp_f32 v222, v222
  v_mul_f32 v149, v149, v221
  v_cvt_pk_bf16_f32 v235, v148, v149
  v_mul_f32 v152, v152, v168
  v_exp_f32 v224, -v152
  v_mfma_f32_32x32x16_bf16 v[186:201], v[62:65], v[138:141], v[186:201]
  ds_read_b128 v[214:217], v12 offset:48128
  buffer_load_dwordx4 v245, s[24:27], s54 offen offset:3072 lds
  v_add_f32 v223, 1.0, v223
  v_rcp_f32 v223, v223
  v_mul_f32 v150, v150, v222
  s_waitcnt lgkmcnt(4)
  v_mfma_f32_32x32x16_bf16 v[186:201], v[66:69], v[178:181], v[186:201]
  ds_read_b128 v[246:249], v12 offset:49152
  v_mul_f32 v153, v153, v168
  v_exp_f32 v225, -v153
  v_add_f32 v224, 1.0, v224
  v_rcp_f32 v224, v224
  v_mfma_f32_32x32x16_bf16 v[186:201], v[70:73], v[182:185], v[186:201]
  ds_read_b128 v[138:141], v12 offset:50176
  v_mul_f32 v151, v151, v223
  v_cvt_pk_bf16_f32 v236, v150, v151
  v_mul_f32 v154, v154, v168
  v_exp_f32 v226, -v154
  v_add_f32 v225, 1.0, v225
  s_waitcnt lgkmcnt(4)
  v_mfma_f32_32x32x16_bf16 v[186:201], v[74:77], v[202:205], v[186:201]
  ds_read_b128 v[178:181], v12 offset:51200
  v_rcp_f32 v225, v225
  v_mul_f32 v152, v152, v224
  v_mul_f32 v155, v155, v168
  v_exp_f32 v227, -v155
  v_mfma_f32_32x32x16_bf16 v[186:201], v[78:81], v[206:209], v[186:201]
  ds_read_b128 v[182:185], v12 offset:52224
  v_add_f32 v226, 1.0, v226
  v_rcp_f32 v226, v226
  v_mul_f32 v153, v153, v225
  v_cvt_pk_bf16_f32 v237, v152, v153
  s_waitcnt lgkmcnt(4)
  v_mfma_f32_32x32x16_bf16 v[186:201], v[82:85], v[210:213], v[186:201]
  ds_read_b128 v[202:205], v12 offset:53248
  s_add_u32 s51, s66, 114688
  v_mfma_f32_32x32x16_bf16 v[186:201], v[86:89], v[214:217], v[186:201]
  ds_read_b128 v[206:209], v12 offset:54272
  buffer_store_dwordx4 v[234:237], v12, s[36:39], s51 offen sc1
  v_mul_f32 v156, v156, v168
  v_exp_f32 v228, -v156
  v_add_f32 v227, 1.0, v227
  v_rcp_f32 v227, v227
  s_waitcnt lgkmcnt(4)
  v_mfma_f32_32x32x16_bf16 v[186:201], v[90:93], v[246:249], v[186:201]
  ds_read_b128 v[210:213], v12 offset:55296
  v_mul_f32 v154, v154, v226
  v_mul_f32 v157, v157, v168
  v_exp_f32 v229, -v157
  v_mfma_f32_32x32x16_bf16 v[186:201], v[94:97], v[138:141], v[186:201]
  ds_read_b128 v[214:217], v12 offset:56320
  s_waitcnt vmcnt(9)
  s_barrier
  buffer_store_dwordx2 v[18:19], v20, s[36:39], s82 offen sc1
  s_waitcnt lgkmcnt(4)
  v_mfma_f32_32x32x16_bf16 v[186:201], v[98:101], v[178:181], v[186:201]
  ds_read_b128 v[246:249], v12 offset:57344
  v_add_f32 v228, 1.0, v228
  v_rcp_f32 v228, v228
  v_mfma_f32_32x32x16_bf16 v[186:201], v[102:105], v[182:185], v[186:201]
  ds_read_b128 v[138:141], v12 offset:58368
  v_mul_f32 v155, v155, v227
  v_cvt_pk_bf16_f32 v238, v154, v155
  v_mul_f32 v158, v158, v168
  v_exp_f32 v230, -v158
  s_waitcnt lgkmcnt(4)
  v_mfma_f32_32x32x16_bf16 v[186:201], v[106:109], v[202:205], v[186:201]
  ds_read_b128 v[178:181], v12 offset:59392
  v_add_f32 v229, 1.0, v229
  v_rcp_f32 v229, v229
  v_mul_f32 v156, v156, v228
  v_mul_f32 v159, v159, v168
  v_mfma_f32_32x32x16_bf16 v[186:201], v[110:113], v[206:209], v[186:201]
  ds_read_b128 v[182:185], v12 offset:60416
  v_exp_f32 v231, -v159
  v_add_f32 v230, 1.0, v230
  v_rcp_f32 v230, v230
  v_mul_f32 v157, v157, v229
  s_waitcnt lgkmcnt(4)
  v_mfma_f32_32x32x16_bf16 v[186:201], v[114:117], v[210:213], v[186:201]
  v_cvt_pk_bf16_f32 v239, v156, v157
  v_mul_f32 v160, v160, v168
  v_exp_f32 v232, -v160
  v_add_f32 v231, 1.0, v231
  v_rcp_f32 v231, v231
  v_mfma_f32_32x32x16_bf16 v[186:201], v[118:121], v[214:217], v[186:201]
  v_mul_f32 v158, v158, v230
  v_mul_f32 v161, v161, v168
  v_exp_f32 v233, -v161
  s_waitcnt lgkmcnt(2)
  v_mfma_f32_32x32x16_bf16 v[186:201], v[122:125], v[246:249], v[186:201]
  v_add_f32 v232, 1.0, v232
  v_rcp_f32 v232, v232
  v_mul_f32 v159, v159, v231
  v_cvt_pk_bf16_f32 v240, v158, v159
  v_add_f32 v233, 1.0, v233
  v_mfma_f32_32x32x16_bf16 v[186:201], v[126:129], v[138:141], v[186:201]
  v_rcp_f32 v233, v233
  v_mul_f32 v160, v160, v232
  v_mul_f32 v161, v161, v233
  v_cvt_pk_bf16_f32 v241, v160, v161
  s_waitcnt lgkmcnt(0)
  v_mfma_f32_32x32x16_bf16 v[186:201], v[130:133], v[178:181], v[186:201]
  ds_read_b64 v[142:143], v169 offset:1536
  buffer_store_dwordx4 v[238:241], v12, s[36:39], s51 offen offset:1024 sc1
  v_mfma_f32_32x32x16_bf16 v[186:201], v[134:137], v[182:185], v[186:201]
  ds_read_b32 v168, v170 offset:384
  s_waitcnt lgkmcnt(1)
  v_mfma_f32_32x32x16_bf16 v[186:201], v[172:175], v[142:145], v[186:201]
  s_waitcnt lgkmcnt(0)
  s_nop 10
  v_mul_f32 v186, v186, v168
  v_exp_f32 v218, -v186
  v_mul_f32 v187, v187, v168
  v_exp_f32 v219, -v187
  v_add_f32 v218, 1.0, v218
  v_rcp_f32 v218, v218
  v_mul_f32 v188, v188, v168
  v_exp_f32 v220, -v188
  v_add_f32 v219, 1.0, v219
  v_rcp_f32 v219, v219
  v_mul_f32 v186, v186, v218
  v_mul_f32 v189, v189, v168
  v_exp_f32 v221, -v189
  v_add_f32 v220, 1.0, v220
  v_rcp_f32 v220, v220
  v_mul_f32 v187, v187, v219
  v_cvt_pk_bf16_f32 v234, v186, v187
  v_mul_f32 v190, v190, v168
  v_exp_f32 v222, -v190
  v_add_f32 v221, 1.0, v221
  v_rcp_f32 v221, v221
  v_mul_f32 v188, v188, v220
  v_mul_f32 v191, v191, v168
  v_exp_f32 v223, -v191
  v_add_f32 v222, 1.0, v222
  v_rcp_f32 v222, v222
  v_mul_f32 v189, v189, v221
  v_cvt_pk_bf16_f32 v235, v188, v189
  v_mul_f32 v192, v192, v168
  v_exp_f32 v224, -v192
  v_add_f32 v223, 1.0, v223
  v_rcp_f32 v223, v223
  v_mul_f32 v190, v190, v222
  v_mul_f32 v193, v193, v168
  v_exp_f32 v225, -v193
  v_add_f32 v224, 1.0, v224
  v_rcp_f32 v224, v224
  v_mul_f32 v191, v191, v223
  v_cvt_pk_bf16_f32 v236, v190, v191
  v_mul_f32 v194, v194, v168
  v_exp_f32 v226, -v194
  v_add_f32 v225, 1.0, v225
  v_rcp_f32 v225, v225
  s_mov_b64 exec, s[68:69]
  buffer_load_dwordx2 v[162:163], v17, s[36:39], s83 offen sc1
  s_mov_b64 exec, -1
  v_mul_f32 v192, v192, v224
  v_mul_f32 v195, v195, v168
  v_exp_f32 v227, -v195
  v_add_f32 v226, 1.0, v226
  v_rcp_f32 v226, v226
  v_mul_f32 v193, v193, v225
  v_cvt_pk_bf16_f32 v237, v192, v193
  s_add_u32 s51, s66, 344064
  buffer_store_dwordx4 v[234:237], v12, s[36:39], s51 offen sc1
  v_mul_f32 v196, v196, v168
  v_exp_f32 v228, -v196
  v_add_f32 v227, 1.0, v227
  v_rcp_f32 v227, v227
  v_mul_f32 v194, v194, v226
  v_mul_f32 v197, v197, v168
  v_exp_f32 v229, -v197
  v_add_f32 v228, 1.0, v228
  v_rcp_f32 v228, v228
  v_mul_f32 v195, v195, v227
  v_cvt_pk_bf16_f32 v238, v194, v195
  v_mul_f32 v198, v198, v168
  v_exp_f32 v230, -v198
  v_add_f32 v229, 1.0, v229
  v_rcp_f32 v229, v229
  v_mul_f32 v196, v196, v228
  v_mul_f32 v199, v199, v168
  v_exp_f32 v231, -v199
  v_add_f32 v230, 1.0, v230
  v_rcp_f32 v230, v230
  v_mul_f32 v197, v197, v229
  v_cvt_pk_bf16_f32 v239, v196, v197
  v_mul_f32 v200, v200, v168
  v_exp_f32 v232, -v200
  v_add_f32 v231, 1.0, v231
  v_rcp_f32 v231, v231
  v_mul_f32 v198, v198, v230
  v_mul_f32 v201, v201, v168
  v_exp_f32 v233, -v201
  v_add_f32 v232, 1.0, v232
  v_rcp_f32 v232, v232
  v_mul_f32 v199, v199, v231
  v_cvt_pk_bf16_f32 v240, v198, v199
  v_add_f32 v233, 1.0, v233
  v_rcp_f32 v233, v233
  v_mul_f32 v200, v200, v232
  v_mul_f32 v201, v201, v233
  v_cvt_pk_bf16_f32 v241, v200, v201
  buffer_store_dwordx4 v[238:241], v12, s[36:39], s51 offen offset:1024 sc1
  s_waitcnt vmcnt(2)
  v_cmp_ne_u64 vcc, s[44:45], v[162:163]
  s_and_b64 vcc, s[68:69], vcc
  s_cbranch_vccz Lok3
  s_mov_b64 exec, s[68:69]
  s_mov_b32 s35, 1048576
Lpl3:
  buffer_load_dwordx2 v[162:163], v17, s[36:39], s83 offen sc1
  s_waitcnt vmcnt(0)
  v_cmp_ne_u64 vcc, s[44:45], v[162:163]
  s_and_b64 vcc, exec, vcc
  s_cbranch_vccz Lpx3
  s_sub_u32 s35, s35, 1
  s_cbranch_scc0 Lpl3
Lpx3:
  s_mov_b64 exec, -1
Lok3:
  buffer_load_dwordx4 v[26:29], v12, s[36:39], s70 offen sc1
  s_add_u32 s52, s42, 81920
  v_add_u32 v171, s52, v22
  buffer_load_dwordx4 v[30:33], v12, s[36:39], s70 offen offset:1024 sc1
  v_add_u32 v176, s52, v23
  buffer_load_dwordx4 v[34:37], v12, s[36:39], s70 offen offset:2048 sc1
  ds_read_b64_tr_b16 a[96:97], v171 offset:0
  ds_read_b64_tr_b16 a[98:99], v171 offset:1024
  buffer_load_dwordx4 v[38:41], v12, s[36:39], s70 offen offset:3072 sc1
  ds_read_b64_tr_b16 a[112:113], v176 offset:0
  ds_read_b64_tr_b16 a[114:115], v176 offset:1024
  s_add_u32 s70, s70, 4096
  buffer_load_dwordx4 v[42:45], v12, s[36:39], s70 offen sc1
  ds_read_b64_tr_b16 a[100:101], v171 offset:2048
  ds_read_b64_tr_b16 a[102:103], v171 offset:3072
  buffer_load_dwordx4 v[46:49], v12, s[36:39], s70 offen offset:1024 sc1
  ds_read_b64_tr_b16 a[116:117], v176 offset:2048
  ds_read_b64_tr_b16 a[118:119], v176 offset:3072
  buffer_load_dwordx4 v[50:53], v12, s[36:39], s70 offen offset:2048 sc1
  ds_read_b64_tr_b16 a[104:105], v171 offset:4096
  ds_read_b64_tr_b16 a[106:107], v171 offset:5120
  buffer_load_dwordx4 v[54:57], v12, s[36:39], s70 offen offset:3072 sc1
  ds_read_b64_tr_b16 a[120:121], v176 offset:4096
  ds_read_b64_tr_b16 a[122:123], v176 offset:5120
  s_add_u32 s70, s70, 4096
  buffer_load_dwordx4 v[58:61], v12, s[36:39], s70 offen sc1
  ds_read_b64_tr_b16 a[108:109], v171 offset:6144
  ds_read_b64_tr_b16 a[110:111], v171 offset:7168
  buffer_load_dwordx4 v[62:65], v12, s[36:39], s70 offen offset:1024 sc1
  ds_read_b64_tr_b16 a[124:125], v176 offset:6144
  ds_read_b64_tr_b16 a[126:127], v176 offset:7168
  buffer_load_dwordx4 v[66:69], v12, s[36:39], s70 offen offset:2048 sc1
  buffer_load_dwordx4 v[70:73], v12, s[36:39], s70 offen offset:3072 sc1
  s_waitcnt vmcnt(11)
  v_mfma_f32_32x32x16_bf16 v[146:161], a[0:3], v[26:29], 0
  v_add_u32 v171, s43, v22
  v_add_u32 v176, s43, v23
  ds_read_b64_tr_b16 a[128:129], v171 offset:0
  v_mfma_f32_32x32x16_bf16 v[186:201], a[16:19], v[26:29], 0
  s_add_u32 s70, s70, 4096
  buffer_load_dwordx4 v[74:77], v12, s[36:39], s70 offen sc1
  ds_read_b64_tr_b16 a[130:131], v171 offset:1024
  s_waitcnt vmcnt(11)
  v_mfma_f32_32x32x16_bf16 v[146:161], a[4:7], v[30:33], v[146:161]
  ds_read_b64_tr_b16 a[144:145], v176 offset:0
  ds_read_b64_tr_b16 a[146:147], v176 offset:1024
  v_mfma_f32_32x32x16_bf16 v[186:201], a[20:23], v[30:33], v[186:201]
  buffer_load_dwordx4 v[78:81], v12, s[36:39], s70 offen offset:1024 sc1
  s_barrier
  buffer_store_dwordx2 v[18:19], v20, s[36:39], s82 offen offset:128 sc1
  buffer_store_dwordx2 v[18:19], v11, s[36:39], s82 offen offset:128 sc1
  ds_read_b64_tr_b16 a[132:133], v171 offset:2048
  s_waitcnt vmcnt(13)
  v_mfma_f32_32x32x16_bf16 v[146:161], a[8:11], v[34:37], v[146:161]
  ds_read_b64_tr_b16 a[134:135], v171 offset:3072
  ds_read_b64_tr_b16 a[148:149], v176 offset:2048
  v_mfma_f32_32x32x16_bf16 v[186:201], a[24:27], v[34:37], v[186:201]
  buffer_load_dwordx4 v[82:85], v12, s[36:39], s70 offen offset:2048 sc1
  ds_read_b64_tr_b16 a[150:151], v176 offset:3072
  s_waitcnt vmcnt(13)
  v_mfma_f32_32x32x16_bf16 v[146:161], a[12:15], v[38:41], v[146:161]
  ds_read_b64_tr_b16 a[136:137], v171 offset:4096
  ds_read_b64_tr_b16 a[138:139], v171 offset:5120
  v_mfma_f32_32x32x16_bf16 v[186:201], a[28:31], v[38:41], v[186:201]
  buffer_load_dwordx4 v[86:89], v12, s[36:39], s70 offen offset:3072 sc1
  ds_read_b64_tr_b16 a[152:153], v176 offset:4096
  s_waitcnt vmcnt(13)
  v_mfma_f32_32x32x16_bf16 v[146:161], a[32:35], v[42:45], v[146:161]
  ds_read_b64_tr_b16 a[154:155], v176 offset:5120
  ds_read_b64_tr_b16 a[140:141], v171 offset:6144
  v_mfma_f32_32x32x16_bf16 v[186:201], a[48:51], v[42:45], v[186:201]
  s_add_u32 s70, s70, 4096
  buffer_load_dwordx4 v[90:93], v12, s[36:39], s70 offen sc1
  ds_read_b64_tr_b16 a[142:143], v171 offset:7168
  s_waitcnt vmcnt(13)
  v_mfma_f32_32x32x16_bf16 v[146:161], a[36:39], v[46:49], v[146:161]
  ds_read_b64_tr_b16 a[156:157], v176 offset:6144
  ds_read_b64_tr_b16 a[158:159], v176 offset:7168
  v_mfma_f32_32x32x16_bf16 v[186:201], a[52:55], v[46:49], v[186:201]
  buffer_load_dwordx4 v[94:97], v12, s[36:39], s70 offen offset:1024 sc1
  s_add_u32 s52, s42, 114688
  s_waitcnt vmcnt(13)
  v_mfma_f32_32x32x16_bf16 v[146:161], a[40:43], v[50:53], v[146:161]
  v_add_u32 v171, s52, v22
  v_add_u32 v176, s52, v23
  ds_read_b64_tr_b16 a[160:161], v171 offset:0
  v_mfma_f32_32x32x16_bf16 v[186:201], a[56:59], v[50:53], v[186:201]
  buffer_load_dwordx4 v[26:29], v12, s[36:39], s70 offen offset:2048 sc1
  ds_read_b64_tr_b16 a[162:163], v171 offset:1024
  s_waitcnt vmcnt(13)
  v_mfma_f32_32x32x16_bf16 v[146:161], a[44:47], v[54:57], v[146:161]
  ds_read_b64_tr_b16 a[176:177], v176 offset:0
  ds_read_b64_tr_b16 a[178:179], v176 offset:1024
  v_mfma_f32_32x32x16_bf16 v[186:201], a[60:63], v[54:57], v[186:201]
  buffer_load_dwordx4 v[30:33], v12, s[36:39], s70 offen offset:3072 sc1
  ds_read_b64_tr_b16 a[164:165], v171 offset:2048
  s_waitcnt vmcnt(13)
  v_mfma_f32_32x32x16_bf16 v[146:161], a[64:67], v[58:61], v[146:161]
  ds_read_b64_tr_b16 a[166:167], v171 offset:3072
  ds_read_b64_tr_b16 a[180:181], v176 offset:2048
  v_mfma_f32_32x32x16_bf16 v[186:201], a[80:83], v[58:61], v[186:201]
  s_add_u32 s70, s70, 4096
  buffer_load_dwordx4 v[34:37], v12, s[36:39], s70 offen sc1
  s_mov_b64 exec, s[68:69]
  buffer_load_dwordx2 v[162:163], v17, s[36:39], s83 offen offset:128 sc1
  s_mov_b64 exec, -1
  ds_read_b64_tr_b16 a[182:183], v176 offset:3072
  s_waitcnt vmcnt(14)
  v_mfma_f32_32x32x16_bf16 v[146:161], a[68:71], v[62:65], v[146:161]
  ds_read_b64_tr_b16 a[168:169], v171 offset:4096
  ds_read_b64_tr_b16 a[170:171], v171 offset:5120
  v_mfma_f32_32x32x16_bf16 v[186:201], a[84:87], v[62:65], v[186:201]
  buffer_load_dwordx4 v[38:41], v12, s[36:39], s70 offen offset:1024 sc1
  ds_read_b64_tr_b16 a[184:185], v176 offset:4096
  s_waitcnt vmcnt(14)
  v_mfma_f32_32x32x16_bf16 v[146:161], a[72:75], v[66:69], v[146:161]
  ds_read_b64_tr_b16 a[186:187], v176 offset:5120
  ds_read_b64_tr_b16 a[172:173], v171 offset:6144
  v_mfma_f32_32x32x16_bf16 v[186:201], a[88:91], v[66:69], v[186:201]
  buffer_load_dwordx4 v[42:45], v12, s[36:39], s70 offen offset:2048 sc1
  ds_read_b64_tr_b16 a[174:175], v171 offset:7168
  s_waitcnt vmcnt(14)
  v_mfma_f32_32x32x16_bf16 v[146:161], a[76:79], v[70:73], v[146:161]
  ds_read_b64_tr_b16 a[188:189], v176 offset:6144
  ds_read_b64_tr_b16 a[190:191], v176 offset:7168
  v_mfma_f32_32x32x16_bf16 v[186:201], a[92:95], v[70:73], v[186:201]
  buffer_load_dwordx4 v[46:49], v12, s[36:39], s70 offen offset:3072 sc1
  s_add_u32 s52, s42, 0
  s_waitcnt vmcnt(14) lgkmcnt(15)
  v_mfma_f32_32x32x16_bf16 v[146:161], a[96:99], v[74:77], v[146:161]
  v_add_u32 v171, s52, v22
  v_add_u32 v176, s52, v23
  ds_read_b64_tr_b16 a[192:193], v171 offset:0
  v_mfma_f32_32x32x16_bf16 v[186:201], a[112:115], v[74:77], v[186:201]
  s_add_u32 s70, s70, 4096
  buffer_load_dwordx4 v[50:53], v12, s[36:39], s70 offen sc1
  ds_read_b64_tr_b16 a[194:195], v171 offset:1024
  s_waitcnt vmcnt(14)
  v_mfma_f32_32x32x16_bf16 v[146:161], a[100:103], v[78:81], v[146:161]
  ds_read_b64_tr_b16 a[208:209], v176 offset:0
  ds_read_b64_tr_b16 a[210:211], v176 offset:1024
  v_mfma_f32_32x32x16_bf16 v[186:201], a[116:119], v[78:81], v[186:201]
  buffer_load_dwordx4 v[54:57], v12, s[36:39], s70 offen offset:1024 sc1
  ds_read_b64_tr_b16 a[196:197], v171 offset:2048
  s_waitcnt vmcnt(12)
  v_mfma_f32_32x32x16_bf16 v[146:161], a[104:107], v[82:85], v[146:161]
  ds_read_b64_tr_b16 a[198:199], v171 offset:3072
  ds_read_b64_tr_b16 a[212:213], v176 offset:2048
  v_mfma_f32_32x32x16_bf16 v[186:201], a[120:123], v[82:85], v[186:201]
  buffer_load_dwordx4 v[58:61], v12, s[36:39], s70 offen offset:2048 sc1
  ds_read_b64_tr_b16 a[214:215], v176 offset:3072
  s_waitcnt vmcnt(12)
  v_mfma_f32_32x32x16_bf16 v[146:161], a[108:111], v[86:89], v[146:161]
  ds_read_b64_tr_b16 a[200:201], v171 offset:4096
  ds_read_b64_tr_b16 a[202:203], v171 offset:5120
  v_mfma_f32_32x32x16_bf16 v[186:201], a[124:127], v[86:89], v[186:201]
  buffer_load_dwordx4 v[62:65], v12, s[36:39], s70 offen offset:3072 sc1
  ds_read_b64_tr_b16 a[216:217], v176 offset:4096
  s_waitcnt vmcnt(12)
  v_mfma_f32_32x32x16_bf16 v[146:161], a[128:131], v[90:93], v[146:161]
  ds_read_b64_tr_b16 a[218:219], v176 offset:5120
  ds_read_b64_tr_b16 a[204:205], v171 offset:6144
  v_mfma_f32_32x32x16_bf16 v[186:201], a[144:147], v[90:93], v[186:201]
  ds_read_b64_tr_b16 a[206:207], v171 offset:7168
  s_waitcnt vmcnt(11)
  v_mfma_f32_32x32x16_bf16 v[146:161], a[132:135], v[94:97], v[146:161]
  ds_read_b64_tr_b16 a[220:221], v176 offset:6144
  ds_read_b64_tr_b16 a[222:223], v176 offset:7168
  v_mfma_f32_32x32x16_bf16 v[186:201], a[148:151], v[94:97], v[186:201]
  s_waitcnt vmcnt(10)
  v_mfma_f32_32x32x16_bf16 v[146:161], a[136:139], v[26:29], v[146:161]
  s_waitcnt vmcnt(7)
  v_cmp_ne_u64 vcc, s[44:45], v[162:163]
  s_and_b64 vcc, s[68:69], vcc
  s_cbranch_vccz Lok4
  s_mov_b64 exec, s[68:69]
  s_mov_b32 s35, 1048576
Lpl4:
  buffer_load_dwordx2 v[162:163], v17, s[36:39], s83 offen offset:128 sc1
  s_waitcnt vmcnt(0)
  v_cmp_ne_u64 vcc, s[44:45], v[162:163]
  s_and_b64 vcc, exec, vcc
  s_cbranch_vccz Lpx4
  s_sub_u32 s35, s35, 1
  s_cbranch_scc0 Lpl4
Lpx4:
  s_mov_b64 exec, -1
Lok4:
  buffer_load_dwordx4 v[66:69], v12, s[36:39], s71 offen sc1
  v_mfma_f32_32x32x16_bf16 v[186:201], a[152:155], v[26:29], v[186:201]
  buffer_load_dwordx4 v[70:73], v12, s[36:39], s71 offen offset:1024 sc1
  v_mfma_f32_32x32x16_bf16 v[146:161], a[140:143], v[30:33], v[146:161]
  buffer_load_dwordx4 v[74:77], v12, s[36:39], s71 offen offset:2048 sc1
  v_mfma_f32_32x32x16_bf16 v[186:201], a[156:159], v[30:33], v[186:201]
  buffer_load_dwordx4 v[78:81], v12, s[36:39], s71 offen offset:3072 sc1
  s_waitcnt lgkmcnt(15)
  v_mfma_f32_32x32x16_bf16 v[146:161], a[160:163], v[34:37], v[146:161]
  v_mfma_f32_32x32x16_bf16 v[186:201], a[176:179], v[34:37], v[186:201]
  s_add_u32 s71, s71, 4096
  buffer_load_dwordx4 v[82:85], v12, s[36:39], s71 offen sc1
  s_waitcnt vmcnt(11)
  v_mfma_f32_32x32x16_bf16 v[146:161], a[164:167], v[38:41], v[146:161]
  v_mfma_f32_32x32x16_bf16 v[186:201], a[180:183], v[38:41], v[186:201]
  buffer_load_dwordx4 v[86:89], v12, s[36:39], s71 offen offset:1024 sc1
  s_waitcnt vmcnt(11)
  v_mfma_f32_32x32x16_bf16 v[146:161], a[168:171], v[42:45], v[146:161]
  s_add_u32 s51, s56, 32768
  v_add_u32 v1, s51, v12
  ds_write_b128 v1, a[224:227] offset:0
  v_mfma_f32_32x32x16_bf16 v[186:201], a[184:187], v[42:45], v[186:201]
  buffer_load_dwordx4 v[90:93], v12, s[36:39], s71 offen offset:2048 sc1
  ds_write_b128 v1, a[228:231] offset:1024
  s_waitcnt vmcnt(11)
  v_mfma_f32_32x32x16_bf16 v[146:161], a[172:175], v[46:49], v[146:161]
  ds_write_b128 v1, a[232:235] offset:2048
  v_mfma_f32_32x32x16_bf16 v[186:201], a[188:191], v[46:49], v[186:201]
  buffer_load_dwordx4 v[94:97], v12, s[36:39], s71 offen offset:3072 sc1
  ds_write_b128 v1, a[236:239] offset:3072
  s_waitcnt vmcnt(11) lgkmcnt(4)
  v_mfma_f32_32x32x16_bf16 v[146:161], a[192:195], v[50:53], v[146:161]
  s_xor_b32 s51, s49, 1
  s_mul_i32 s51, s51, 3072
  s_add_u32 s51, s51, 0
  v_add_u32 v139, s51, v12
  s_xor_b32 s51, s49, 2
  v_mfma_f32_32x32x16_bf16 v[186:201], a[208:211], v[50:53], v[186:201]
  s_add_u32 s71, s71, 4096
  buffer_load_dwordx4 v[26:29], v12, s[36:39], s71 offen sc1
  s_mul_i32 s51, s51, 3072
  s_waitcnt vmcnt(11)
  v_mfma_f32_32x32x16_bf16 v[146:161], a[196:199], v[54:57], v[146:161]
  s_add_u32 s51, s51, 1024
  v_add_u32 v140, s51, v12
  s_xor_b32 s51, s49, 3
  s_mul_i32 s51, s51, 3072
  s_add_u32 s51, s51, 2048
  v_mfma_f32_32x32x16_bf16 v[186:201], a[212:215], v[54:57], v[186:201]
  buffer_load_dwordx4 v[30:33], v12, s[36:39], s71 offen offset:1024 sc1
  v_add_u32 v141, s51, v12
  s_waitcnt vmcnt(11)
  v_mfma_f32_32x32x16_bf16 v[146:161], a[200:203], v[58:61], v[146:161]
  s_mul_i32 s51, s49, 3072
  v_add_u32 v8, s51, v12
  s_lshl_b32 s51, s78, 13
  s_lshl_b32 s54, s77, 11
  s_add_u32 s51, s51, s54
  v_mfma_f32_32x32x16_bf16 v[186:201], a[216:219], v[58:61], v[186:201]
  buffer_load_dwordx4 v[34:37], v12, s[36:39], s71 offen offset:2048 sc1
  s_add_u32 s51, s51, 32768
  s_waitcnt vmcnt(11)
  v_mfma_f32_32x32x16_bf16 v[146:161], a[204:207], v[62:65], v[146:161]
  v_add_u32 v21, s51, v12
  v_mul_u32_u24 v24, 896, v13
  v_lshl_add_u32 v24, v14, 4, v24
  s_lshl_b32 s51, s33, 1
  s_add_u32 s51, s51, s74
  v_mfma_f32_32x32x16_bf16 v[186:201], a[220:223], v[62:65], v[186:201]
  buffer_load_dwordx4 v[38:41], v12, s[36:39], s71 offen offset:3072 sc1
  s_mul_i32 s51, s51, 57344
  s_waitcnt vmcnt(11)
  v_mfma_f32_32x32x16_bf16 v[202:217], a[0:3], v[66:69], 0
  s_lshl_b32 s54, s75, 7
  s_add_u32 s54, s54, s51
  s_lshl_b32 s51, s49, 5
  s_add_u32 s54, s54, s51
  s_mov_b32 s60, 0x3f317218
  v_mfma_f32_32x32x16_bf16 v[218:233], a[16:19], v[66:69], 0
  s_add_u32 s71, s71, 4096
  buffer_load_dwordx4 v[42:45], v12, s[36:39], s71 offen sc1
  s_waitcnt vmcnt(11)
  v_mfma_f32_32x32x16_bf16 v[202:217], a[4:7], v[70:73], v[202:217]
  v_mfma_f32_32x32x16_bf16 v[218:233], a[20:23], v[70:73], v[218:233]
  buffer_load_dwordx4 v[46:49], v12, s[36:39], s71 offen offset:1024 sc1
  s_waitcnt vmcnt(11)
  v_mfma_f32_32x32x16_bf16 v[202:217], a[8:11], v[74:77], v[202:217]
  v_cvt_pk_bf16_f32 v110, v154, v155
  v_cvt_pk_bf16_f32 v111, v156, v157
  v_cvt_pk_bf16_f32 v112, v158, v159
  v_cvt_pk_bf16_f32 v113, v160, v161
  ds_write_b128 v139, v[110:113] offset:49152
  v_mfma_f32_32x32x16_bf16 v[218:233], a[24:27], v[74:77], v[218:233]
  buffer_load_dwordx4 v[50:53], v12, s[36:39], s71 offen offset:2048 sc1
  v_cvt_pk_bf16_f32 v114, v186, v187
  s_waitcnt vmcnt(11)
  v_mfma_f32_32x32x16_bf16 v[202:217], a[12:15], v[78:81], v[202:217]
  v_cvt_pk_bf16_f32 v115, v188, v189
  v_cvt_pk_bf16_f32 v116, v190, v191
  v_cvt_pk_bf16_f32 v117, v192, v193
  ds_write_b128 v140, v[114:117] offset:49152
  v_mfma_f32_32x32x16_bf16 v[218:233], a[28:31], v[78:81], v[218:233]
  buffer_load_dwordx4 v[54:57], v12, s[36:39], s71 offen offset:3072 sc1
  v_cvt_pk_bf16_f32 v118, v194, v195
  s_waitcnt vmcnt(11)
  v_mfma_f32_32x32x16_bf16 v[202:217], a[32:35], v[82:85], v[202:217]
  v_cvt_pk_bf16_f32 v119, v196, v197
  v_cvt_pk_bf16_f32 v120, v198, v199
  v_cvt_pk_bf16_f32 v121, v200, v201
  ds_write_b128 v141, v[118:121] offset:49152
  v_mfma_f32_32x32x16_bf16 v[218:233], a[48:51], v[82:85], v[218:233]
  s_add_u32 s71, s71, 4096
  buffer_load_dwordx4 v[58:61], v12, s[36:39], s71 offen sc1
  s_waitcnt vmcnt(11)
  v_mfma_f32_32x32x16_bf16 v[202:217], a[36:39], v[86:89], v[202:217]
  v_mfma_f32_32x32x16_bf16 v[218:233], a[52:55], v[86:89], v[218:233]
  buffer_load_dwordx4 v[62:65], v12, s[36:39], s71 offen offset:1024 sc1
  s_waitcnt lgkmcnt(0)
  s_waitcnt vmcnt(11)
  v_mfma_f32_32x32x16_bf16 v[202:217], a[40:43], v[90:93], v[202:217]
  s_barrier
  ds_read_b128 v[122:125], v21 offset:0
  v_mfma_f32_32x32x16_bf16 v[218:233], a[56:59], v[90:93], v[218:233]
  buffer_load_dwordx4 v[66:69], v12, s[36:39], s71 offen offset:2048 sc1
  ds_read_b128 v[126:129], v21 offset:1024
  s_waitcnt vmcnt(11)
  v_mfma_f32_32x32x16_bf16 v[202:217], a[44:47], v[94:97], v[202:217]
  ds_read_b128 v[130:133], v21 offset:4096
  ds_read_b128 v[134:137], v21 offset:5120
  ds_read_b128 v[98:101], v8 offset:49152
  v_mfma_f32_32x32x16_bf16 v[218:233], a[60:63], v[94:97], v[218:233]
  buffer_load_dwordx4 v[70:73], v12, s[36:39], s71 offen offset:3072 sc1
  ds_read_b128 v[102:105], v8 offset:50176
  s_waitcnt vmcnt(11)
  v_mfma_f32_32x32x16_bf16 v[202:217], a[64:67], v[26:29], v[202:217]
  ds_read_b128 v[106:109], v8 offset:51200
  s_waitcnt lgkmcnt(2)
  v_lshlrev_b32 v1, 16, v98
  v_and_b32 v2, 0xffff0000, v98
  v_add_f32 v146, v146, v1
  v_mfma_f32_32x32x16_bf16 v[218:233], a[80:83], v[26:29], v[218:233]
  s_add_u32 s71, s71, 4096
  buffer_load_dwordx4 v[74:77], v12, s[36:39], s71 offen sc1
  v_add_f32 v147, v147, v2
  s_waitcnt vmcnt(11)
  v_mfma_f32_32x32x16_bf16 v[202:217], a[68:71], v[30:33], v[202:217]
  v_lshlrev_b32 v1, 16, v99
  v_and_b32 v2, 0xffff0000, v99
  v_add_f32 v148, v148, v1
  v_add_f32 v149, v149, v2
  v_lshlrev_b32 v1, 16, v100
  v_and_b32 v2, 0xffff0000, v100
  v_mfma_f32_32x32x16_bf16 v[218:233], a[84:87], v[30:33], v[218:233]
  buffer_load_dwordx4 v[78:81], v12, s[36:39], s71 offen offset:1024 sc1
  v_add_f32 v150, v150, v1
  s_waitcnt vmcnt(11)
  v_mfma_f32_32x32x16_bf16 v[202:217], a[72:75], v[34:37], v[202:217]
  v_add_f32 v151, v151, v2
  v_lshlrev_b32 v1, 16, v101
  v_and_b32 v2, 0xffff0000, v101
  v_add_f32 v152, v152, v1
  v_add_f32 v153, v153, v2
  s_waitcnt lgkmcnt(1)
  v_lshlrev_b32 v1, 16, v102
  v_mfma_f32_32x32x16_bf16 v[218:233], a[88:91], v[34:37], v[218:233]
  buffer_load_dwordx4 v[82:85], v12, s[36:39], s71 offen offset:2048 sc1
  v_and_b32 v2, 0xffff0000, v102
  s_waitcnt vmcnt(11)
  v_mfma_f32_32x32x16_bf16 v[202:217], a[76:79], v[38:41], v[202:217]
  v_add_f32 v146, v146, v1
  v_add_f32 v147, v147, v2
  v_lshlrev_b32 v1, 16, v103
  v_and_b32 v2, 0xffff0000, v103
  v_add_f32 v148, v148, v1
  v_add_f32 v149, v149, v2
  v_mfma_f32_32x32x16_bf16 v[218:233], a[92:95], v[38:41], v[218:233]
  buffer_load_dwordx4 v[86:89], v12, s[36:39], s71 offen offset:3072 sc1
  v_lshlrev_b32 v1, 16, v104
  s_waitcnt vmcnt(11)
  v_mfma_f32_32x32x16_bf16 v[202:217], a[96:99], v[42:45], v[202:217]
  v_and_b32 v2, 0xffff0000, v104
  v_add_f32 v150, v150, v1
  v_add_f32 v151, v151, v2
  v_lshlrev_b32 v1, 16, v105
  v_and_b32 v2, 0xffff0000, v105
  v_add_f32 v152, v152, v1
  v_mfma_f32_32x32x16_bf16 v[218:233], a[112:115], v[42:45], v[218:233]
  s_add_u32 s71, s71, 4096
  buffer_load_dwordx4 v[90:93], v12, s[36:39], s71 offen sc1
  v_add_f32 v153, v153, v2
  s_waitcnt vmcnt(11)
  v_mfma_f32_32x32x16_bf16 v[202:217], a[100:103], v[46:49], v[202:217]
  s_waitcnt lgkmcnt(0)
  v_lshlrev_b32 v1, 16, v106
  v_and_b32 v2, 0xffff0000, v106
  v_add_f32 v146, v146, v1
  v_add_f32 v147, v147, v2
  v_lshlrev_b32 v1, 16, v107
  v_and_b32 v2, 0xffff0000, v107
  v_mfma_f32_32x32x16_bf16 v[218:233], a[116:119], v[46:49], v[218:233]
  buffer_load_dwordx4 v[94:97], v12, s[36:39], s71 offen offset:1024 sc1
  v_add_f32 v148, v148, v1
  s_waitcnt vmcnt(11)
  v_mfma_f32_32x32x16_bf16 v[202:217], a[104:107], v[50:53], v[202:217]
  v_add_f32 v149, v149, v2
  v_lshlrev_b32 v1, 16, v108
  v_and_b32 v2, 0xffff0000, v108
  v_add_f32 v150, v150, v1
  v_add_f32 v151, v151, v2
  v_lshlrev_b32 v1, 16, v109
  v_mfma_f32_32x32x16_bf16 v[218:233], a[120:123], v[50:53], v[218:233]
  buffer_load_dwordx4 v[26:29], v12, s[36:39], s71 offen offset:2048 sc1
  v_and_b32 v2, 0xffff0000, v109
  s_waitcnt vmcnt(11)
  v_mfma_f32_32x32x16_bf16 v[202:217], a[108:111], v[54:57], v[202:217]
  v_add_f32 v152, v152, v1
  v_add_f32 v153, v153, v2
  v_fmac_f32 v122, s60, v146
  v_fmac_f32 v123, s60, v147
  v_fmac_f32 v124, s60, v148
  v_fmac_f32 v125, s60, v149
  v_mfma_f32_32x32x16_bf16 v[218:233], a[124:127], v[54:57], v[218:233]
  buffer_load_dwordx4 v[30:33], v12, s[36:39], s71 offen offset:3072 sc1
  v_fmac_f32 v126, s60, v150
  s_waitcnt vmcnt(11)
  v_mfma_f32_32x32x16_bf16 v[202:217], a[128:131], v[58:61], v[202:217]
  v_fmac_f32 v127, s60, v151
  v_fmac_f32 v128, s60, v152
  v_fmac_f32 v129, s60, v153
  v_cvt_pk_bf16_f32 v110, v122, v123
  v_cvt_pk_bf16_f32 v111, v124, v125
  v_cvt_pk_bf16_f32 v112, v126, v127
  v_mfma_f32_32x32x16_bf16 v[218:233], a[144:147], v[58:61], v[218:233]
  v_cvt_pk_bf16_f32 v113, v128, v129
  s_waitcnt vmcnt(10)
  v_mfma_f32_32x32x16_bf16 v[202:217], a[132:135], v[62:65], v[202:217]
  v_permlane32_swap_b32 v110, v112
  v_permlane32_swap_b32 v111, v113
  buffer_store_dwordx4 v[110:113], v24, s[28:31], s54 offen
  v_mfma_f32_32x32x16_bf16 v[218:233], a[148:151], v[62:65], v[218:233]
  s_waitcnt vmcnt(10)
  v_mfma_f32_32x32x16_bf16 v[202:217], a[136:139], v[66:69], v[202:217]
  v_mfma_f32_32x32x16_bf16 v[218:233], a[152:155], v[66:69], v[218:233]
  s_waitcnt vmcnt(9)
  v_mfma_f32_32x32x16_bf16 v[202:217], a[140:143], v[70:73], v[202:217]
  v_mfma_f32_32x32x16_bf16 v[218:233], a[156:159], v[70:73], v[218:233]
  s_waitcnt vmcnt(8)
  v_mfma_f32_32x32x16_bf16 v[202:217], a[160:163], v[74:77], v[202:217]
  v_mfma_f32_32x32x16_bf16 v[218:233], a[176:179], v[74:77], v[218:233]
  s_waitcnt vmcnt(7)
  v_mfma_f32_32x32x16_bf16 v[202:217], a[164:167], v[78:81], v[202:217]
  v_mfma_f32_32x32x16_bf16 v[218:233], a[180:183], v[78:81], v[218:233]
  s_waitcnt vmcnt(6)
  v_mfma_f32_32x32x16_bf16 v[202:217], a[168:171], v[82:85], v[202:217]
  v_mfma_f32_32x32x16_bf16 v[218:233], a[184:187], v[82:85], v[218:233]
  s_waitcnt vmcnt(5)
  v_mfma_f32_32x32x16_bf16 v[202:217], a[172:175], v[86:89], v[202:217]
  v_mfma_f32_32x32x16_bf16 v[218:233], a[188:191], v[86:89], v[218:233]
  s_waitcnt vmcnt(4)
  v_mfma_f32_32x32x16_bf16 v[202:217], a[192:195], v[90:93], v[202:217]
  v_mfma_f32_32x32x16_bf16 v[218:233], a[208:211], v[90:93], v[218:233]
  s_waitcnt vmcnt(3)
  v_mfma_f32_32x32x16_bf16 v[202:217], a[196:199], v[94:97], v[202:217]
  v_mfma_f32_32x32x16_bf16 v[218:233], a[212:215], v[94:97], v[218:233]
  s_waitcnt vmcnt(2)
  v_mfma_f32_32x32x16_bf16 v[202:217], a[200:203], v[26:29], v[202:217]
  v_mfma_f32_32x32x16_bf16 v[218:233], a[216:219], v[26:29], v[218:233]
  s_waitcnt vmcnt(1)
  v_mfma_f32_32x32x16_bf16 v[202:217], a[204:207], v[30:33], v[202:217]
  v_mfma_f32_32x32x16_bf16 v[218:233], a[220:223], v[30:33], v[218:233]
  s_nop 10
  v_cvt_pk_bf16_f32 v110, v210, v211
  v_cvt_pk_bf16_f32 v111, v212, v213
  v_cvt_pk_bf16_f32 v112, v214, v215
  v_cvt_pk_bf16_f32 v113, v216, v217
  ds_write_b128 v139, v[110:113] offset:0
  v_cvt_pk_bf16_f32 v114, v218, v219
  v_cvt_pk_bf16_f32 v115, v220, v221
  v_cvt_pk_bf16_f32 v116, v222, v223
  v_cvt_pk_bf16_f32 v117, v224, v225
  ds_write_b128 v140, v[114:117] offset:0
  v_cvt_pk_bf16_f32 v118, v226, v227
  v_cvt_pk_bf16_f32 v119, v228, v229
  v_cvt_pk_bf16_f32 v120, v230, v231
  v_cvt_pk_bf16_f32 v121, v232, v233
  ds_write_b128 v141, v[118:121] offset:0
  s_waitcnt lgkmcnt(0)
  s_barrier
  ds_read_b128 v[98:101], v8 offset:0
  ds_read_b128 v[102:105], v8 offset:1024
  ds_read_b128 v[106:109], v8 offset:2048
  s_waitcnt lgkmcnt(2)
  v_dot2c_f32_bf16 v202, s63, v98
  v_dot2c_f32_bf16 v203, s64, v98
  v_dot2c_f32_bf16 v204, s63, v99
  v_dot2c_f32_bf16 v205, s64, v99
  v_dot2c_f32_bf16 v206, s63, v100
  v_dot2c_f32_bf16 v207, s64, v100
  v_dot2c_f32_bf16 v208, s63, v101
  v_dot2c_f32_bf16 v209, s64, v101
  s_waitcnt lgkmcnt(1)
  v_dot2c_f32_bf16 v202, s63, v102
  v_dot2c_f32_bf16 v203, s64, v102
  v_dot2c_f32_bf16 v204, s63, v103
  v_dot2c_f32_bf16 v205, s64, v103
  v_dot2c_f32_bf16 v206, s63, v104
  v_dot2c_f32_bf16 v207, s64, v104
  v_dot2c_f32_bf16 v208, s63, v105
  v_dot2c_f32_bf16 v209, s64, v105
  s_waitcnt lgkmcnt(0)
  v_dot2c_f32_bf16 v202, s63, v106
  v_dot2c_f32_bf16 v203, s64, v106
  v_dot2c_f32_bf16 v204, s63, v107
  v_dot2c_f32_bf16 v205, s64, v107
  v_dot2c_f32_bf16 v206, s63, v108
  v_dot2c_f32_bf16 v207, s64, v108
  v_dot2c_f32_bf16 v208, s63, v109
  v_dot2c_f32_bf16 v209, s64, v109
  s_add_u32 s54, s54, 28672
  v_fmac_f32 v130, s60, v202
  v_fmac_f32 v131, s60, v203
  v_fmac_f32 v132, s60, v204
  v_fmac_f32 v133, s60, v205
  v_fmac_f32 v134, s60, v206
  v_fmac_f32 v135, s60, v207
  v_fmac_f32 v136, s60, v208
  v_fmac_f32 v137, s60, v209
  v_cvt_pk_bf16_f32 v110, v130, v131
  v_cvt_pk_bf16_f32 v111, v132, v133
  v_cvt_pk_bf16_f32 v112, v134, v135
  v_cvt_pk_bf16_f32 v113, v136, v137
  s_nop 0
  v_permlane32_swap_b32 v110, v112
  v_permlane32_swap_b32 v111, v113
  buffer_store_dwordx4 v[110:113], v24, s[28:31], s54 offen
Lhh_done:
Lhh_end:
)ASM"
               :
               : "s"(__builtin_amdgcn_kernarg_segment_ptr()), "s"(blockIdx.x), "v"(threadIdx.x)
               : "memory", "vcc", "scc", "v1", "v2", "v3", "v4", "v5", "v6", "v7", "v8", "v9", "v10", "v11", "v12", "v13",
                 "v14", "v15", "v16", "v17", "v18", "v19", "v20", "v21", "v22", "v23", "v24", "v25", "v26", "v27", "v28", "v29",
                 "v30", "v31", "v32", "v33", "v34", "v35", "v36", "v37", "v38", "v39", "v40", "v41", "v42", "v43", "v44", "v45",
                 "v46", "v47", "v48", "v49", "v50", "v51", "v52", "v53", "v54", "v55", "v56", "v57", "v58", "v59", "v60", "v61",
                 "v62", "v63", "v64", "v65", "v66", "v67", "v68", "v69", "v70", "v71", "v72", "v73", "v74", "v75", "v76", "v77",
                 "v78", "v79", "v80", "v81", "v82", "v83", "v84", "v85", "v86", "v87", "v88", "v89", "v90", "v91", "v92", "v93",
                 "v94", "v95", "v96", "v97", "v98", "v99", "v100", "v101", "v102", "v103", "v104", "v105", "v106", "v107", "v108", "v109",
                 "v110", "v111", "v112", "v113", "v114", "v115", "v116", "v117", "v118", "v119", "v120", "v121", "v122", "v123", "v124", "v125",
                 "v126", "v127", "v128", "v129", "v130", "v131", "v132", "v133", "v134", "v135", "v136", "v137", "v138", "v139", "v140", "v141",
                 "v142", "v143", "v144", "v145", "v146", "v147", "v148", "v149", "v150", "v151", "v152", "v153", "v154", "v155", "v156", "v157",
                 "v158", "v159", "v160", "v161", "v162", "v163", "v164", "v165", "v166", "v167", "v168", "v169", "v170", "v171", "v172", "v173",
                 "v174", "v175", "v176", "v177", "v178", "v179", "v180", "v181", "v182", "v183", "v184", "v185", "v186", "v187", "v188", "v189",
                 "v190", "v191", "v192", "v193", "v194", "v195", "v196", "v197", "v198", "v199", "v200", "v201", "v202", "v203", "v204", "v205",
                 "v206", "v207", "v208", "v209", "v210", "v211", "v212", "v213", "v214", "v215", "v216", "v217", "v218", "v219", "v220", "v221",
                 "v222", "v223", "v224", "v225", "v226", "v227", "v228", "v229", "v230", "v231", "v232", "v233", "v234", "v235", "v236", "v237",
                 "v238", "v239", "v240", "v241", "v242", "v243", "v244", "v245", "v246", "v247", "v248", "v249", "v250", "v251", "v252", "v253",
                 "v254", "v255", "a0", "a1", "a2", "a3", "a4", "a5", "a6", "a7", "a8", "a9", "a10", "a11", "a12", "a13",
                 "a14", "a15", "a16", "a17", "a18", "a19", "a20", "a21", "a22", "a23", "a24", "a25", "a26", "a27", "a28", "a29",
                 "a30", "a31", "a32", "a33", "a34", "a35", "a36", "a37", "a38", "a39", "a40", "a41", "a42", "a43", "a44", "a45",
                 "a46", "a47", "a48", "a49", "a50", "a51", "a52", "a53", "a54", "a55", "a56", "a57", "a58", "a59", "a60", "a61",
                 "a62", "a63", "a64", "a65", "a66", "a67", "a68", "a69", "a70", "a71", "a72", "a73", "a74", "a75", "a76", "a77",
                 "a78", "a79", "a80", "a81", "a82", "a83", "a84", "a85", "a86", "a87", "a88", "a89", "a90", "a91", "a92", "a93",
                 "a94", "a95", "a96", "a97", "a98", "a99", "a100", "a101", "a102", "a103", "a104", "a105", "a106", "a107", "a108", "a109",
                 "a110", "a111", "a112", "a113", "a114", "a115", "a116", "a117", "a118", "a119", "a120", "a121", "a122", "a123", "a124", "a125",
                 "a126", "a127", "a128", "a129", "a130", "a131", "a132", "a133", "a134", "a135", "a136", "a137", "a138", "a139", "a140", "a141",
                 "a142", "a143", "a144", "a145", "a146", "a147", "a148", "a149", "a150", "a151", "a152", "a153", "a154", "a155", "a156", "a157",
                 "a158", "a159", "a160", "a161", "a162", "a163", "a164", "a165", "a166", "a167", "a168", "a169", "a170", "a171", "a172", "a173",
                 "a174", "a175", "a176", "a177", "a178", "a179", "a180", "a181", "a182", "a183", "a184", "a185", "a186", "a187", "a188", "a189",
                 "a190", "a191", "a192", "a193", "a194", "a195", "a196", "a197", "a198", "a199", "a200", "a201", "a202", "a203", "a204", "a205",
                 "a206", "a207", "a208", "a209", "a210", "a211", "a212", "a213", "a214", "a215", "a216", "a217", "a218", "a219", "a220", "a221",
                 "a222", "a223", "a224", "a225", "a226", "a227", "a228", "a229", "a230", "a231", "a232", "a233", "a234", "a235", "a236", "a237",
                 "a238", "a239", "a240", "a241", "a242", "a243", "a244", "a245", "a246", "a247", "a248", "a249", "a250", "a251", "a252", "a253",
                 "a254", "a255", "s4", "s5", "s6", "s7", "s8", "s9", "s10", "s11", "s12", "s13", "s14", "s15", "s16", "s17",
                 "s18", "s19", "s20", "s21", "s22", "s23", "s24", "s25", "s26", "s27", "s28", "s29", "s30", "s31", "s33", "s34",
                 "s35", "s36", "s37", "s38", "s39", "s40", "s41", "s42", "s43", "s44", "s45", "s46", "s47", "s48", "s49", "s50",
                 "s51", "s52", "s53", "s54", "s55", "s56", "s57", "s58", "s59", "s60", "s61", "s62", "s63", "s64", "s65", "s66",
                 "s67", "s68", "s69", "s70", "s71", "s72", "s73", "s74", "s75", "s76", "s77", "s78", "s79", "s80", "s81", "s82",
                 "s83", "s84", "s85", "s86", "s87", "s88", "s89", "s90", "s91", "s92", "s93", "s94", "s95", "s96", "s97", "s98",
                 "s99");
}

extern "C" void kernel_launch(void* const* d_in, const int* in_sizes, int n_in, void* d_out, int out_size, void* d_ws, size_t ws_size,
                              hipStream_t stream) {
  constexpr int kLds = 160 * 1024;
  (void)hipFuncSetAttribute((const void*)hh_block_kernel, hipFuncAttributeMaxDynamicSharedMemorySize, kLds);
  hh_block_kernel<<<256, 256, kLds, stream>>>(d_in[0], d_in[1], d_in[2], d_in[3], d_in[5], d_in[6], d_out, d_ws);
}
```
